```python
import torch

def _softmax(x, axis=-1):
    return torch.softmax(x, dim=axis)
def _gelu(x):
    return torch.nn.functional.gelu(x, approximate="tanh")
def _silu(x):
    return torch.nn.functional.silu(x)
def _sigmoid(x):
    return torch.sigmoid(x)
def _layernorm(x, axis=-1, eps=1e-5):
    m = x.mean(dim=axis, keepdim=True)
    v = x.var(dim=axis, keepdim=True, unbiased=False)
    return (x - m) / torch.sqrt(v + eps)
def _scatter_add(tgt, idx, src, axis=0):
    return tgt.clone().index_add_(axis, idx, src)
def _segment_sum(src, seg, n):
    out = torch.zeros((n,) + src.shape[1:], dtype=src.dtype)
    return out.index_add_(0, seg, src)
def _pad(x, pw):
    flat = tuple(v for pair in reversed(pw) for v in pair)
    return torch.nn.functional.pad(x, flat)

kernel_name = 'standalone__mixer_s_mlp__prompt'

def setup_inputs():

    _w = (lambda a: (lambda t: t.to(torch.bfloat16) if t.is_floating_point() else t)(torch.as_tensor(a)))
    B, S, D, F = 1, 2048, 1024, 3584
    return {
        "x":  _w((lambda *s: torch.randn(*s, dtype=torch.bfloat16) * 0.4)(B, S, D)),
        "w1": _w((lambda *s: torch.randn(*s, dtype=torch.bfloat16) * 3.2)(D, D) * (D ** -0.5)),
        "w2": _w((lambda *s: torch.randn(*s, dtype=torch.bfloat16) * 3.2)(D, D) * (D ** -0.5)),
        "w3": _w((lambda *s: torch.randn(*s, dtype=torch.bfloat16) * 3.2)(D, F) * (D ** -0.5)),
        "w4": _w((lambda *s: torch.randn(*s, dtype=torch.bfloat16) * 0.8)(F, D) * (F ** -0.5)),
    }

def reference(x, w1, w2, w3, w4):
    B, S, D = x.shape
    h = _gelu(_layernorm(x, axis=-1) @ w1)
    c = D // 4
    g0 = _pad(h[:, :, 0*c:1*c], ((0, 0), (1, 0), (0, 0)))[:, :S, :]
    g1 = _pad(h[:, :, 1*c:2*c], ((0, 0), (0, 1), (0, 0)))[:, 1:, :]
    g2 = _pad(h[:, :, 2*c:3*c], ((0, 0), (2, 0), (0, 0)))[:, :S, :]
    g3 = _pad(h[:, :, 3*c:   ], ((0, 0), (0, 2), (0, 0)))[:, 2:, :]
    hs = torch.concatenate([g0, g1, g2, g3], axis=-1) @ w2
    x1 = x + hs
    h2 = _gelu(_layernorm(x1, axis=-1) @ w3) @ w4
    return x1 + h2
```

```cpp
#include <hip/hip_runtime.h>
#include <hip/hip_bf16.h>

typedef __attribute__((ext_vector_type(8))) __bf16 bf16x8;
typedef __attribute__((ext_vector_type(4))) __bf16 bf16x4;
typedef __attribute__((ext_vector_type(2))) __bf16 bf16x2;
typedef __attribute__((ext_vector_type(4))) short s16x4;
typedef __attribute__((ext_vector_type(8))) short s16x8;
typedef __attribute__((ext_vector_type(4))) float f32x4;
typedef __attribute__((ext_vector_type(2))) float f32x2;
typedef __attribute__((ext_vector_type(4))) unsigned u32x4;
typedef __attribute__((ext_vector_type(2))) unsigned u32x2;
#define LAS __attribute__((address_space(3)))
#define GAS __attribute__((address_space(1)))
#define DEVI __device__ __forceinline__

constexpr int S_ = 2048, D_ = 1024, F_ = 3584;
constexpr size_t WS_G = 0;
constexpr size_t WS_X1 = WS_G + (size_t)S_ * D_ * 2;
constexpr size_t WS_H2 = WS_X1 + (size_t)S_ * D_ * 2;
constexpr size_t WS_ST = WS_H2 + (size_t)S_ * F_ * 2;
constexpr size_t WS_CS = WS_ST + (size_t)S_ * 16 * 8;
constexpr size_t WS_EX = WS_CS + (size_t)16 * F_ * 4;
constexpr size_t WS_FL = WS_EX + (size_t)256 * 32768;

template <int MODE> struct Cfg;
template <> struct Cfg<1> { static constexpr int BN = 64, NT = 16, LDW = 1024, NS = 6, TM = 2, TN = 4, KD = 1024; };
template <> struct Cfg<2> { static constexpr int BN = 64, NT = 16, LDW = 1024, NS = 6, TM = 2, TN = 4, KD = 1024; };
template <> struct Cfg<3> { static constexpr int BN = 224, NT = 16, LDW = 3584, NS = 3, TM = 4, TN = 7, KD = 1024; };
template <> struct Cfg<4> { static constexpr int BN = 128, NT = 28, LDW = 1024, NS = 4, TM = 4, TN = 4, KD = 3584; };

#if defined(__HIP_DEVICE_COMPILE__)
DEVI __amdgpu_buffer_rsrc_t mk_rsrc(const void* p, unsigned bytes) {
  return __builtin_amdgcn_make_buffer_rsrc((void*)p, 0, bytes, 0x00020000);
}
template <int N> DEVI void wait_vm() { __builtin_amdgcn_s_waitcnt(0x0F70 | (N & 15) | ((N >> 4) << 14)); }
DEVI void wait_lgkm0() { __builtin_amdgcn_s_waitcnt(0xC07F); }
DEVI void bar() { asm volatile("s_barrier" ::: "memory"); }
DEVI unsigned pk2(float a, float b) {
  bf16x2 v = {(__bf16)a, (__bf16)b};
  return __builtin_bit_cast(unsigned, v);
}
DEVI float lo16(unsigned u) { return __builtin_bit_cast(float, u << 16); }
DEVI float hi16(unsigned u) { return __builtin_bit_cast(float, u & 0xffff0000u); }
DEVI float xor16(float v) { return __shfl_xor(v, 16, 64); }
DEVI float xor32(float v) { return __shfl_xor(v, 32, 64); }
DEVI f32x2 gelu2(f32x2 x) {
  const float c0 = -2.0f * 0.7978845608028654f * 1.4426950408889634f, c1 = c0 * 0.044715f;
  f32x2 z = x * (x * x * c1 + c0);
  f32x2 e = {__builtin_amdgcn_exp2f(z.x), __builtin_amdgcn_exp2f(z.y)};
  e = e + 1.0f;
  f32x2 r = {__builtin_amdgcn_rcpf(e.x), __builtin_amdgcn_rcpf(e.y)};
  return x * r;
}
template <int BN> DEVI int swz(int rho) {
  return BN == 64 ? 2 * ((rho >> 1) & 3) : BN == 128 ? 2 * (rho & 7) : 2 * ((rho >> 2) & 1);
}
template <int KD> DEVI unsigned fo_block(int tg, int kb) {
  return (unsigned)((((tg >> 3) * (KD / 64) + (kb >> 1)) * 16 + (tg & 7) * 2 + (kb & 1)) * 1024);
}
#endif

template <int MODE>
__global__ __launch_bounds__(512, 2) void gemm_k(const char* __restrict__ Ap, const char* __restrict__ Wp,
                                                 const char* __restrict__ Xp, const char* __restrict__ W3p,
                                                 char* __restrict__ ws, char* __restrict__ outp) {
#if defined(__HIP_DEVICE_COMPILE__)
  using C = Cfg<MODE>;
  constexpr int BN = C::BN, NT = C::NT, LDW = C::LDW, NS = C::NS, TM = C::TM, TN = C::TN, KD = C::KD;
  constexpr int SB = 16384 + BN * 128, NLB = BN / 32, NL = 4 + NLB;
  extern __shared__ __attribute__((aligned(16))) char sm[];
  const int bid = blockIdx.x, xcd = bid & 7, loc = bid >> 3;
  int tm, tn, ks = 0;
  if (MODE <= 2) { tm = (xcd >> 1) * 4 + (loc >> 3); tn = (xcd & 1) * 8 + (loc & 7); }
  else if (MODE == 3) { tm = (xcd >> 2) * 8 + (loc >> 2); tn = (xcd & 3) * 4 + (loc & 3); }
  else { tm = (xcd >> 1) * 4 + (loc >> 3); tn = loc & 7; ks = xcd & 1; }
  const int tid = threadIdx.x, l = tid & 63, w = __builtin_amdgcn_readfirstlane(tid >> 6);
  const int g = l >> 4, lt = l & 15;

  if (w >= 4) {
    const int lw = w - 4;
    const __amdgpu_buffer_rsrc_t ra = mk_rsrc(Ap, (unsigned)S_ * KD * 2), rw = mk_rsrc(Wp, (unsigned)(MODE == 4 ? F_ : D_) * LDW * 2);
    unsigned va[4], vb[NLB];
#pragma unroll
    for (int i = 0; i < 4; i++) {
      const int c = (lw * 4 + i) * 64 + l;
      if (MODE == 1) { const int r = c >> 3, p = c & 7; va[i] = (unsigned)((tm * 128 + r) * D_ * 2 + ((p ^ (r & 7)) << 4)); }
      else va[i] = (unsigned)((tm * (KD / 64) + ks * NT) * 16384 + c * 16);
    }
#pragma unroll
    for (int i = 0; i < NLB; i++) {
      const int c = (lw * NLB + i) * 64 + l, r = c / (BN / 8), p = c % (BN / 8);
      const int k = MODE == 1 ? (r & 32) + 8 * ((r >> 2) & 3) + 4 * ((r >> 4) & 1) + (r & 3) : r;
      vb[i] = (unsigned)(((ks * NT * 64 + k) * LDW + tn * BN) * 2 + ((p ^ swz<BN>(r)) << 4));
    }
    constexpr unsigned SA = MODE == 1 ? 128 : 16384, SW = 64 * LDW * 2;
    auto issue = [&](int t, int slot) {
      LAS char* base = (LAS char*)sm + slot * SB;
#pragma unroll
      for (int i = 0; i < 4; i++)
        __builtin_amdgcn_raw_ptr_buffer_load_lds(ra, base + (lw * 4 + i) * 1024, 16, va[i], t * SA, 0, 0);
#pragma unroll
      for (int i = 0; i < NLB; i++)
        __builtin_amdgcn_raw_ptr_buffer_load_lds(rw, base + 16384 + (lw * NLB + i) * 1024, 16, vb[i], t * SW, 0, 0);
    };
#pragma unroll
    for (int t = 0; t < NS - 1; t++) issue(t, t);
    int slot = NS - 1;
    for (int t = 0; t < NT - (NS - 1); t++) {
      wait_vm<(NS - 2) * NL>();
      bar();
      issue(t + NS - 1, slot);
      slot = slot == NS - 1 ? 0 : slot + 1;
    }
    if (NS >= 6) { wait_vm<(NS >= 6 ? 4 : 0) * NL>(); bar(); }
    if (NS >= 5) { wait_vm<(NS >= 5 ? 3 : 0) * NL>(); bar(); }
    if (NS >= 4) { wait_vm<(NS >= 4 ? 2 : 0) * NL>(); bar(); }
    wait_vm<NL>(); bar();
    wait_vm<0>(); bar();
    return;
  }

  const int wm = MODE <= 2 ? w : (w >> 1), wn = MODE <= 2 ? 0 : (w & 1);
  const int tg0 = tm * 8 + wm * TM;
  const int n0 = tn * BN + wn * TN * 16;
  float* csl = (float*)(sm + NS * SB);

  f32x4 acc[TM][TN];
#pragma unroll
  for (int i = 0; i < TM; i++)
#pragma unroll
    for (int j = 0; j < TN; j++) acc[i][j] = f32x4{0.f, 0.f, 0.f, 0.f};
  f32x4 cs[MODE == 1 ? TN : 1];
  float s1[TM], s2[TM];
  u32x2 xr[MODE == 2 ? TM : 1][MODE == 2 ? TN : 1];
  u32x4 x1r[MODE == 4 ? 2 : 1][MODE == 4 ? 2 : 1];
#pragma unroll
  for (int i = 0; i < TM; i++) s1[i] = s2[i] = 0.f;

  if (MODE == 1) {
#pragma unroll
    for (int j = 0; j < TN; j++) cs[j] = f32x4{0.f, 0.f, 0.f, 0.f};
    if (tid == 0) *(GAS unsigned*)(ws + WS_FL + bid * 128) = 0u;
    const int rg = l / 7, c = l - rg * 7;
    float a[8] = {0.f, 0.f, 0.f, 0.f, 0.f, 0.f, 0.f, 0.f};
    const int col = tn * 224 + w * 56 + c * 8;
    if (l < 56) {
      u32x4 v[8];
#pragma unroll
      for (int r = 0; r < 8; r++) v[r] = *(const GAS u32x4*)(W3p + ((size_t)(tm * 64 + rg * 8 + r) * F_ + col) * 2);
#pragma unroll
      for (int r = 0; r < 8; r++)
#pragma unroll
        for (int e = 0; e < 4; e++) { a[2 * e] += lo16(v[r][e]); a[2 * e + 1] += hi16(v[r][e]); }
    }
#pragma unroll
    for (int e = 0; e < 8; e++) {
      a[e] += __shfl_down(a[e], 28, 64); a[e] += __shfl_down(a[e], 14, 64); a[e] += __shfl_down(a[e], 7, 64);
    }
    if (l < 7) {
      GAS f32x4* o = (GAS f32x4*)(ws + WS_CS + ((size_t)tm * F_ + col) * 4);
      o[0] = f32x4{a[0], a[1], a[2], a[3]}; o[1] = f32x4{a[4], a[5], a[6], a[7]};
    }
  }
  if (MODE == 2) {
#pragma unroll
    for (int i = 0; i < TM; i++)
#pragma unroll
      for (int j = 0; j < TN; j++)
        xr[i][j] = *(const GAS u32x2*)(Xp + ((size_t)((tg0 + i) * 16 + lt) * D_ + n0 + j * 16 + g * 4) * 2);
  }
  if (MODE == 3) {
    if (tid < 224) {
      float s = 0.f, v[16];
#pragma unroll
      for (int r = 0; r < 16; r++) v[r] = *(const GAS float*)(ws + WS_CS + ((size_t)r * F_ + tn * 224 + tid) * 4);
#pragma unroll
      for (int r = 0; r < 16; r++) s += v[r];
      csl[tid] = s;
    }
#pragma unroll
    for (int i = 0; i < TM; i++) {
      const GAS f32x4* p = (const GAS f32x4*)(ws + WS_ST + (size_t)((tg0 + i) * 16 + lt) * 128 + g * 32);
      const f32x4 u = p[0], v = p[1];
      s1[i] = (u[0] + u[2]) + (v[0] + v[2]); s2[i] = (u[1] + u[3]) + (v[1] + v[3]);
    }
  }
  if (MODE == 4) {
#pragma unroll
    for (int i = 0; i < 2; i++)
#pragma unroll
      for (int j = 0; j < 2; j++)
        x1r[i][j] = *(const GAS u32x4*)(ws + WS_X1 + fo_block<D_>(tg0 + ks * 2 + i, (n0 >> 5) + j) + l * 16);
  }

  unsigned oa[MODE == 1 ? 2 : 1], ob[TN];
  if (MODE == 1) {
    oa[0] = (wm * 32 + lt) * 128 + ((g ^ (lt & 7)) << 4);
    oa[MODE == 1 ? 1 : 0] = (wm * 32 + lt) * 128 + (((4 + g) ^ (lt & 7)) << 4);
  } else oa[0] = wm * TM * 2048 + l * 16;
  {
    const int rho = 4 * g + (lt >> 2), p = l & 3;
#pragma unroll
    for (int j = 0; j < TN; j++)
      ob[j] = (BN == 224 && j >= 2) ? ob[j & 1] + (j >> 1) * 64
                                    : 16384 + rho * BN * 2 + ((((wn * TN + j) * 2) ^ swz<BN>(rho)) + (p >> 1)) * 16 + (p & 1) * 8;
  }
  bf16x8 fa[2][TM], fb[2][TN];
  auto rd = [&](int st, unsigned base) {
    const LAS char* s = (const LAS char*)sm + base;
#pragma unroll
    for (int j = 0; j < TN; j++) {
      const LAS char* q = s + ob[j] + st * 32 * BN * 2;
      const s16x4 u = __builtin_amdgcn_ds_read_tr16_b64_v4i16((LAS s16x4*)q);
      const s16x4 v = __builtin_amdgcn_ds_read_tr16_b64_v4i16((LAS s16x4*)(q + 16 * BN * 2));
      fb[st][j] = __builtin_bit_cast(bf16x8, __builtin_shufflevector(u, v, 0, 1, 2, 3, 4, 5, 6, 7));
      if (j == 0) {
#pragma unroll
        for (int i = 0; i < TM; i++)
          fa[st][i] = MODE == 1 ? *(const LAS bf16x8*)(s + oa[MODE == 1 ? st : 0] + i * 2048)
                                : *(const LAS bf16x8*)(s + oa[0] + i * 2048 + st * 1024);
      }
    }
  };
  const bf16x8 ones = {1.f, 1.f, 1.f, 1.f, 1.f, 1.f, 1.f, 1.f};
  auto mm = [&](int st) {
#pragma unroll
    for (int j = 0; j < TN; j++) {
#pragma unroll
      for (int i = 0; i < TM; i++) acc[i][j] = __builtin_amdgcn_mfma_f32_16x16x32_bf16(fb[st][j], fa[st][i], acc[i][j], 0, 0, 0);
      if (MODE == 1) cs[j] = __builtin_amdgcn_mfma_f32_16x16x32_bf16(fb[st][j], ones, cs[j], 0, 0, 0);
    }
    if (MODE == 1) {
      const bf16x2 o2 = {1.f, 1.f};
#pragma unroll
      for (int i = 0; i < TM; i++)
#pragma unroll
        for (int e = 0; e < 4; e++) {
          const bf16x2 v = {fa[st][i][2 * e], fa[st][i][2 * e + 1]};
          s1[i] = __builtin_amdgcn_fdot2_f32_bf16(v, o2, s1[i], false);
          s2[i] = __builtin_amdgcn_fdot2_f32_bf16(v, v, s2[i], false);
        }
    }
  };
  auto mix = [&]() {
    constexpr int NR = TM + 2 * TN, NM = TM * TN + (MODE == 1 ? TN : 0);
#pragma unroll
    for (int i = 0; i < (NR + 1) / 2; i++) {
      __builtin_amdgcn_sched_group_barrier(0x008, 1, 0);
      __builtin_amdgcn_sched_group_barrier(0x100, 2, 0);
      if (MODE == 1) __builtin_amdgcn_sched_group_barrier(0x002, 2, 0);
    }
    __builtin_amdgcn_sched_group_barrier(0x008, NM - (NR + 1) / 2, 0);
    __builtin_amdgcn_sched_barrier(0);
  };
  bar();
  rd(0, 0); rd(1, 0);
  __builtin_amdgcn_sched_barrier(0);
  mm(0);
  unsigned base = SB;
  for (int t = 1; t < NT; t++) {
    __builtin_amdgcn_sched_barrier(0);
    wait_lgkm0();
    bar();
    __builtin_amdgcn_sched_barrier(0);
    rd(0, base); mm(1); mix();
    rd(1, base); mm(0); mix();
    base = base == (NS - 1) * SB ? 0 : base + SB;
  }
  mm(1);

  if (MODE == 1 || MODE == 3) {
    const int q = tn >> 2, sh = (q & 1) ? -1 - (q >> 1) : 1 + (q >> 1);
#pragma unroll
    for (int i = 0; i < TM; i++) {
      float a = s1[i], b = s2[i];
      a += xor16(a); b += xor16(b); a += xor32(a); b += xor32(b);
      const float mean = a * (1.f / D_), var = fmaxf(b * (1.f / D_) - mean * mean, 0.f);
      const float rs = __builtin_amdgcn_rsqf(var + 1e-5f), nm = -mean * rs;
      unsigned pk[TN][2];
#pragma unroll
      for (int j = 0; j < TN; j++) {
        const f32x4 c = MODE == 1 ? cs[MODE == 1 ? j : 0] : *(const f32x4*)(csl + wn * 112 + j * 16 + g * 4);
        const f32x4 y = acc[i][j] * rs + c * nm;
        const f32x2 u = gelu2(f32x2{y[0], y[1]}), v = gelu2(f32x2{y[2], y[3]});
        pk[j][0] = pk2(u.x, u.y); pk[j][1] = pk2(v.x, v.y);
      }
      if (MODE == 1) {
        const int td = (tg0 + i) * 16 + lt + sh, tw = td & (S_ - 1);
        const unsigned keep = td == tw ? 0xffffffffu : 0u;
#pragma unroll
        for (int j = 0; j < TN; j += 2)
          *(GAS u32x4*)(ws + WS_G + fo_block<D_>(tw >> 4, (n0 >> 5) + (j >> 1)) + (g * 16 + (tw & 15)) * 16) =
              u32x4{pk[j][0] & keep, pk[j][1] & keep, pk[j + 1][0] & keep, pk[j + 1][1] & keep};
      } else {
        const int kb0 = tn * 7 + wn * 4;
        if (wn == 0) {
#pragma unroll
          for (int j = 0; j < 3; j++)
            *(GAS u32x4*)(ws + WS_H2 + fo_block<F_>(tg0 + i, kb0 + j) + l * 16) = u32x4{pk[2 * j][0], pk[2 * j][1], pk[2 * j + 1][0], pk[2 * j + 1][1]};
          *(GAS u32x2*)(ws + WS_H2 + fo_block<F_>(tg0 + i, kb0 + 3) + l * 16) = u32x2{pk[TN - 1][0], pk[TN - 1][1]};
        } else {
          *(GAS u32x2*)(ws + WS_H2 + fo_block<F_>(tg0 + i, kb0 - 1) + l * 16 + 8) = u32x2{pk[0][0], pk[0][1]};
#pragma unroll
          for (int j = 0; j < 3; j++)
            *(GAS u32x4*)(ws + WS_H2 + fo_block<F_>(tg0 + i, kb0 + j) + l * 16) =
                u32x4{pk[(2 * j + 1) % TN][0], pk[(2 * j + 1) % TN][1], pk[(2 * j + 2) % TN][0], pk[(2 * j + 2) % TN][1]};
        }
      }
    }
  }
  if (MODE == 2) {
#pragma unroll
    for (int i = 0; i < TM; i++) {
      unsigned pk[TN][2];
      float a = 0.f, b = 0.f;
#pragma unroll
      for (int j = 0; j < TN; j++)
#pragma unroll
        for (int e = 0; e < 2; e++) {
          const unsigned h = pk2(acc[i][j][2 * e], acc[i][j][2 * e + 1]), x = xr[MODE == 2 ? i : 0][MODE == 2 ? j : 0][e];
          const unsigned y = pk2(lo16(x) + lo16(h), hi16(x) + hi16(h));
          pk[j][e] = y;
          a += lo16(y) + hi16(y); b += lo16(y) * lo16(y) + hi16(y) * hi16(y);
        }
#pragma unroll
      for (int j = 0; j < TN; j += 2)
        *(GAS u32x4*)(ws + WS_X1 + fo_block<D_>(tg0 + i, (n0 >> 5) + (j >> 1)) + l * 16) = u32x4{pk[j][0], pk[j][1], pk[j + 1][0], pk[j + 1][1]};
      a += xor16(a); b += xor16(b); a += xor32(a); b += xor32(b);
      if (g == 0) *(GAS f32x2*)(ws + WS_ST + (size_t)((tg0 + i) * 16 + lt) * 128 + tn * 8) = f32x2{a, b};
    }
  }
  if (MODE == 4) {
    const __amdgpu_buffer_rsrc_t rx = mk_rsrc(ws + WS_EX, 256 * 32768);
    f32x4 mine[2][TN];
#pragma unroll
    for (int i = 0; i < 2; i++)
#pragma unroll
      for (int j = 0; j < TN; j++) {
        const f32x4 snd = ks ? acc[i][j] : acc[(2 + i) % TM][j];
        mine[i][j] = ks ? acc[(2 + i) % TM][j] : acc[i][j];
        __builtin_amdgcn_raw_buffer_store_b128(__builtin_bit_cast(u32x4, snd), rx, bid * 32768 + w * 8192 + (i * 4 + j) * 1024 + l * 16, 0, 16);
      }
    asm volatile("s_waitcnt vmcnt(0)" ::: "memory");
    bar();
    if (w == 0) {
      if (l == 0) __hip_atomic_store((GAS unsigned*)(ws + WS_FL + bid * 128), 1u, __ATOMIC_RELAXED, __HIP_MEMORY_SCOPE_AGENT);
      while (__hip_atomic_load((GAS unsigned*)(ws + WS_FL + (bid ^ 1) * 128), __ATOMIC_RELAXED, __HIP_MEMORY_SCOPE_AGENT) != 1u)
        __builtin_amdgcn_s_sleep(1);
    }
    bar();
    u32x4 oth[2][TN];
#pragma unroll
    for (int i = 0; i < 2; i++)
#pragma unroll
      for (int j = 0; j < TN; j++)
        oth[i][j] = __builtin_amdgcn_raw_buffer_load_b128(rx, (bid ^ 1) * 32768 + w * 8192 + (i * 4 + j) * 1024 + l * 16, 0, 16);
#pragma unroll
    for (int i = 0; i < 2; i++)
#pragma unroll
      for (int jp = 0; jp < 2; jp++) {
        unsigned y[4];
#pragma unroll
        for (int e = 0; e < 4; e++) {
          const int j = (jp * 2 + (e >> 1)) % TN, r = (e & 1) * 2;
          const f32x4 o = __builtin_bit_cast(f32x4, oth[i][j]);
          const unsigned h = pk2(mine[i][j][r] + o[r], mine[i][j][r + 1] + o[r + 1]), x = x1r[MODE == 4 ? i : 0][MODE == 4 ? jp : 0][e];
          y[e] = pk2(lo16(x) + lo16(h), hi16(x) + hi16(h));
        }
        const u32x2 p0 = __builtin_amdgcn_permlane16_swap(y[0], y[2], false, false);
        const u32x2 p1 = __builtin_amdgcn_permlane16_swap(y[1], y[3], false, false);
        const int col = n0 + jp * 32 + (g & 1) * 16 + (g >> 1) * 8;
        *(GAS u32x4*)(outp + ((size_t)((tg0 + ks * 2 + i) * 16 + lt) * D_ + col) * 2) = u32x4{p0[0], p1[0], p0[1], p1[1]};
      }
  }
#endif
}

template <int MODE> static void launch(const void* A, const void* W, const void* X, const void* W3, void* ws, void* out, hipStream_t st) {
  constexpr int lds = Cfg<MODE>::NS * (16384 + Cfg<MODE>::BN * 128) + 1024;
  (void)hipFuncSetAttribute((const void*)gemm_k<MODE>, hipFuncAttributeMaxDynamicSharedMemorySize, lds);
  gemm_k<MODE><<<256, 512, lds, st>>>((const char*)A, (const char*)W, (const char*)X, (const char*)W3, (char*)ws, (char*)out);
}

extern "C" void kernel_launch(void* const* d_in, const int* in_sizes, int n_in, void* d_out, int out_size, void* d_ws,
                              size_t ws_size, hipStream_t stream) {
  char* ws = (char*)d_ws;
  launch<1>(d_in[0], d_in[1], d_in[0], d_in[3], ws, d_out, stream);
  launch<2>(ws + WS_G, d_in[2], d_in[0], d_in[3], ws, d_out, stream);
  launch<3>(ws + WS_X1, d_in[3], d_in[0], d_in[3], ws, d_out, stream);
  launch<4>(ws + WS_H2, d_in[4], d_in[0], d_in[3], ws, d_out, stream);
}
```

```cpp
#include <hip/hip_runtime.h>
#include <hip/hip_bf16.h>
#include <type_traits>

typedef __attribute__((ext_vector_type(8))) __bf16 bf16x8;
typedef __attribute__((ext_vector_type(4))) __bf16 bf16x4;
typedef __attribute__((ext_vector_type(2))) __bf16 bf16x2;
typedef __attribute__((ext_vector_type(4))) short s16x4;
typedef __attribute__((ext_vector_type(8))) short s16x8;
typedef __attribute__((ext_vector_type(4))) float f32x4;
typedef __attribute__((ext_vector_type(2))) float f32x2;
typedef __attribute__((ext_vector_type(4))) unsigned u32x4;
typedef __attribute__((ext_vector_type(2))) unsigned u32x2;
#define LAS __attribute__((address_space(3)))
#define GAS __attribute__((address_space(1)))
#define DEVI __device__ __forceinline__

constexpr int S_ = 2048, D_ = 1024, F_ = 3584;
constexpr size_t WS_G = 0;
constexpr size_t WS_X1 = WS_G + (size_t)S_ * D_ * 2;
constexpr size_t WS_H2 = WS_X1 + (size_t)S_ * D_ * 2;
constexpr size_t WS_ST = WS_H2 + (size_t)S_ * F_ * 2;
constexpr size_t WS_CS = WS_ST + (size_t)S_ * 16 * 8;
constexpr size_t WS_EX = WS_CS + (size_t)16 * F_ * 4;
constexpr size_t WS_FL = WS_EX + (size_t)256 * 32768;

#define ST(i)
template <int MODE> struct Cfg;
template <> struct Cfg<1> { static constexpr int BN = 64, NT = 16, LDW = 1024, NS = 6, TM = 2, TN = 4, KD = 1024; };
template <> struct Cfg<2> { static constexpr int BN = 64, NT = 16, LDW = 1024, NS = 6, TM = 2, TN = 4, KD = 1024; };
template <> struct Cfg<3> { static constexpr int BN = 224, NT = 16, LDW = 3584, NS = 3, TM = 4, TN = 7, KD = 1024; };
template <> struct Cfg<4> { static constexpr int BN = 128, NT = 28, LDW = 1024, NS = 4, TM = 4, TN = 4, KD = 3584; };

#if defined(__HIP_DEVICE_COMPILE__)
DEVI __amdgpu_buffer_rsrc_t mk_rsrc(const void* p, unsigned bytes) {
  return __builtin_amdgcn_make_buffer_rsrc((void*)p, 0, bytes, 0x00020000);
}
template <int N> DEVI void wait_vm() { __builtin_amdgcn_s_waitcnt(0x0F70 | (N & 15) | ((N >> 4) << 14)); }
DEVI void wait_lgkm0() { __builtin_amdgcn_s_waitcnt(0xC07F); }
DEVI void bar() { asm volatile("s_barrier" ::: "memory"); }
DEVI unsigned pk2(float a, float b) {
  bf16x2 v = {(__bf16)a, (__bf16)b};
  return __builtin_bit_cast(unsigned, v);
}
DEVI float lo16(unsigned u) { return __builtin_bit_cast(float, u << 16); }
DEVI float hi16(unsigned u) { return __builtin_bit_cast(float, u & 0xffff0000u); }
DEVI float xor16(float v) { return __shfl_xor(v, 16, 64); }
DEVI float xor32(float v) { return __shfl_xor(v, 32, 64); }
DEVI f32x2 gelu2(f32x2 x) {
  const float c0 = -2.0f * 0.7978845608028654f * 1.4426950408889634f, c1 = c0 * 0.044715f;
  f32x2 z = x * (x * x * c1 + c0);
  f32x2 e = {__builtin_amdgcn_exp2f(z.x), __builtin_amdgcn_exp2f(z.y)};
  e = e + 1.0f;
  f32x2 r = {__builtin_amdgcn_rcpf(e.x), __builtin_amdgcn_rcpf(e.y)};
  return x * r;
}
template <int I, int N, class F> DEVI void sfor(F&& f) {
  if constexpr (I < N) { f(std::integral_constant<int, I>{}); sfor<I + 1, N>(f); }
}
constexpr int n_issued(int t, int NS, int NT) {
  int a = 2 + 2 * (t + 1), b = t + NS;
  a = a < b ? a : b;
  return a < NT ? a : NT;
}
#define STAUX 0
template <int BN> DEVI int swz(int rho) {
  return BN == 64 ? 2 * ((rho >> 1) & 3) : BN == 128 ? 2 * (rho & 7) : 2 * ((rho >> 2) & 1);
}
template <int KD> DEVI unsigned fo_block(int tg, int kb) {
  return (unsigned)((((tg >> 3) * (KD / 64) + (kb >> 1)) * 16 + (tg & 7) * 2 + (kb & 1)) * 1024);
}
#endif

template <int MODE>
__global__ __launch_bounds__(512, 2) void gemm_k(const char* __restrict__ Ap, const char* __restrict__ Wp,
                                                 const char* __restrict__ Xp, const char* __restrict__ W3p, const char* __restrict__ Pfp,
                                                 char* __restrict__ ws, char* __restrict__ outp) {
#if defined(__HIP_DEVICE_COMPILE__)
  using C = Cfg<MODE>;
  constexpr int BN = C::BN, NT = C::NT, LDW = C::LDW, NS = C::NS, TM = C::TM, TN = C::TN, KD = C::KD;
  constexpr int SB = 16384 + BN * 128, NLB = BN / 32, NL = 4 + NLB;
  extern __shared__ __attribute__((aligned(16))) char sm[];
  const int bid = blockIdx.x, xcd = bid & 7, loc = bid >> 3;
  int tm, tn, ks = 0;
  if (MODE <= 2) { tm = (xcd >> 1) * 4 + (loc >> 3); tn = (xcd & 1) * 8 + (loc & 7); }
  else if (MODE == 3) { tm = (xcd >> 2) * 8 + (loc >> 2); tn = (xcd & 3) * 4 + (loc & 3); }
  else { tm = (xcd >> 1) * 4 + (loc >> 3); tn = loc & 7; ks = xcd & 1; }
  const int tid = threadIdx.x, l = tid & 63, w = __builtin_amdgcn_readfirstlane(tid >> 6);
  const int g = l >> 4, lt = l & 15;

  ST(0);
  if (w >= 4) {
    const int lw = w - 4;
    const __amdgpu_buffer_rsrc_t ra = mk_rsrc(Ap, (unsigned)S_ * KD * 2), rw = mk_rsrc(Wp, (unsigned)(MODE == 4 ? F_ : D_) * LDW * 2);
    unsigned va[4], vb[NLB];
#pragma unroll
    for (int i = 0; i < 4; i++) {
      const int c = (lw * 4 + i) * 64 + l;
      if (MODE == 1) { const int r = c >> 3, p = c & 7; va[i] = (unsigned)((tm * 128 + r) * D_ * 2 + ((p ^ (r & 7)) << 4)); }
      else va[i] = (unsigned)((tm * (KD / 64) + ks * NT) * 16384 + c * 16);
    }
#pragma unroll
    for (int i = 0; i < NLB; i++) {
      const int c = (lw * NLB + i) * 64 + l, r = c / (BN / 8), p = c % (BN / 8);
      const int k = MODE == 1 ? (r & 32) + 8 * ((r >> 2) & 3) + 4 * ((r >> 4) & 1) + (r & 3) : r;
      vb[i] = (unsigned)(((ks * NT * 64 + k) * LDW + tn * BN) * 2 + ((p ^ swz<BN>(r)) << 4));
    }
    constexpr unsigned SA = MODE == 1 ? 128 : 16384, SW = 64 * LDW * 2;
#define R3A 0
#define R3B 0
#define R4A 0
#define R4B 0
#define R1A 1
#define R1B 2
    const int rot = MODE <= 2 ? (R1A * (tn & 7) + R1B * (tm & 3)) % NT : MODE == 3 ? (R3A * (tn & 3) + R3B * (tm & 7)) % NT : (R4A * tn + R4B * (tm & 3)) % NT;
    auto issue = [&](int ts, int slot) {
      const int t = ts + rot >= NT ? ts + rot - NT : ts + rot;
      LAS char* base = (LAS char*)sm + slot * SB;
#pragma unroll
      for (int i = 0; i < 4; i++)
        __builtin_amdgcn_raw_ptr_buffer_load_lds(ra, base + (lw * 4 + i) * 1024, 16, va[i], t * SA, 0, 0);
#pragma unroll
      for (int i = 0; i < NLB; i++)
        __builtin_amdgcn_raw_ptr_buffer_load_lds(rw, base + 16384 + (lw * NLB + i) * 1024, 16, vb[i], t * SW, 0, 0);
    };
    issue(0, 0); issue(1, 1);
    ST(1);
    constexpr int NP = MODE == 1 ? 10 : MODE == 2 ? 7 : 0, TP = NT - NS;
    u32x4 pf[NP + 1];
    const int rg = l / 7, w3col = tn * 224 + lw * 56 + (l - rg * 7) * 8;
    sfor<0, NT>([&](auto tc) {
      constexpr int t = tc.value, i0 = n_issued(t - 1, NS, NT), i1 = n_issued(t, NS, NT);
      wait_vm<(i0 - (t + 1)) * NL + (t > TP ? NP : 0)>();
      bar();
      if constexpr (i1 > i0) issue(i0, i0 % NS);
      if constexpr (i1 > i0 + 1) issue(i0 + 1, (i0 + 1) % NS);
      if constexpr (t == TP && MODE == 1) {
        __builtin_amdgcn_sched_barrier(0);
#pragma unroll
        for (int r = 0; r < 8; r++) pf[r] = *(const GAS u32x4*)(W3p + ((size_t)(tm * 64 + (rg & 7) * 8 + r) * F_ + w3col) * 2);
#pragma unroll
        for (int r = 0; r < 2; r++) pf[8 + r] = *(const GAS u32x4*)(Pfp + (size_t)bid * 8192 + r * 4096 + (tid - 256) * 16);
      }
      if constexpr (t == TP && MODE == 2) {
        __builtin_amdgcn_sched_barrier(0);
#pragma unroll
        for (int r = 0; r < 7; r++) pf[r] = *(const GAS u32x4*)(Pfp + (size_t)bid * 28672 + r * 4096 + (tid - 256) * 16);
      }
    });
#pragma unroll
    for (int r = 0; r < NP; r++) asm volatile("" : "+v"(pf[r]));
    if constexpr (MODE == 1) {
      float a[8] = {0.f, 0.f, 0.f, 0.f, 0.f, 0.f, 0.f, 0.f};
#pragma unroll
      for (int r = 0; r < 8; r++)
#pragma unroll
        for (int e = 0; e < 4; e++) { a[2 * e] += lo16(pf[r][e]); a[2 * e + 1] += hi16(pf[r][e]); }
#pragma unroll
      for (int e = 0; e < 8; e++) {
        a[e] += __shfl_down(a[e], 28, 64); a[e] += __shfl_down(a[e], 14, 64); a[e] += __shfl_down(a[e], 7, 64);
      }
      if (l < 7) {
        GAS f32x4* o = (GAS f32x4*)(ws + WS_CS + (size_t)(tm * F_ + w3col) * 4);
        o[0] = f32x4{a[0], a[1], a[2], a[3]}; o[1] = f32x4{a[4], a[5], a[6], a[7]};
      }
    }
    ST(2);
    return;
  }

  const int wm = MODE <= 2 ? w : (w >> 1), wn = MODE <= 2 ? 0 : (w & 1);
  const int tg0 = tm * 8 + wm * TM;
  const int n0 = tn * BN + wn * TN * 16;
  float* csl = (float*)(sm + NS * SB);

  f32x4 acc[TM][TN];
#pragma unroll
  for (int i = 0; i < TM; i++)
#pragma unroll
    for (int j = 0; j < TN; j++) acc[i][j] = f32x4{0.f, 0.f, 0.f, 0.f};
  f32x4 cs = {0.f, 0.f, 0.f, 0.f}, sx[2], sq[2];
  sx[0] = sx[1] = sq[0] = sq[1] = cs;
  float s1[TM], s2[TM];
  u32x2 xr[MODE == 2 ? TM : 1][MODE == 2 ? TN : 1];
  u32x4 x1r[MODE == 4 ? 2 : 1][MODE == 4 ? 2 : 1];
#pragma unroll
  for (int i = 0; i < TM; i++) s1[i] = s2[i] = 0.f;

  if (MODE == 1 && tid == 0) *(GAS unsigned*)(ws + WS_FL + bid * 128) = 0u;
  if (MODE == 2) {
#pragma unroll
    for (int i = 0; i < TM; i++)
#pragma unroll
      for (int j = 0; j < TN; j++)
        xr[i][j] = *(const GAS u32x2*)(Xp + ((size_t)((tg0 + i) * 16 + lt) * D_ + n0 + j * 16 + g * 4) * 2);
  }
  if (MODE == 3) {
    if (tid < 224) {
      float s = 0.f, v[16];
#pragma unroll
      for (int r = 0; r < 16; r++) v[r] = *(const GAS float*)(ws + WS_CS + ((size_t)r * F_ + tn * 224 + tid) * 4);
#pragma unroll
      for (int r = 0; r < 16; r++) s += v[r];
      csl[tid] = s;
    }
#pragma unroll
    for (int i = 0; i < TM; i++) {
      const GAS f32x4* p = (const GAS f32x4*)(ws + WS_ST + (size_t)((tg0 + i) * 16 + lt) * 128 + g * 32);
      const f32x4 u = p[0], v = p[1];
      s1[i] = (u[0] + u[2]) + (v[0] + v[2]); s2[i] = (u[1] + u[3]) + (v[1] + v[3]);
    }
  }
  if (MODE == 4) {
#pragma unroll
    for (int i = 0; i < 2; i++)
#pragma unroll
      for (int j = 0; j < 2; j++)
        x1r[i][j] = *(const GAS u32x4*)(ws + WS_X1 + fo_block<D_>(tg0 + ks * 2 + i, (n0 >> 5) + j) + l * 16);
  }

  unsigned oa[MODE == 1 ? 2 : 1], ob[TN];
  if (MODE == 1) {
    oa[0] = (wm * 32 + lt) * 128 + ((g ^ (lt & 7)) << 4);
    oa[MODE == 1 ? 1 : 0] = (wm * 32 + lt) * 128 + (((4 + g) ^ (lt & 7)) << 4);
  } else oa[0] = wm * TM * 2048 + l * 16;
  {
    const int rho = 4 * g + (lt >> 2), p = l & 3;
#pragma unroll
    for (int j = 0; j < TN; j++)
      ob[j] = (BN == 224 && j >= 2) ? ob[j & 1] + (j >> 1) * 64
                                    : 16384 + rho * BN * 2 + ((((MODE == 1 ? j ^ w : wn * TN + j) * 2) ^ swz<BN>(rho)) + (p >> 1)) * 16 + (p & 1) * 8;
  }
  bf16x8 fa[2][TM], fb[2][TN];
  auto rd = [&](int st, unsigned base) {
    const LAS char* s = (const LAS char*)sm + base;
#pragma unroll
    for (int j = 0; j < TN; j++) {
      const LAS char* q = s + ob[j] + st * 32 * BN * 2;
      const s16x4 u = __builtin_amdgcn_ds_read_tr16_b64_v4i16((LAS s16x4*)q);
      const s16x4 v = __builtin_amdgcn_ds_read_tr16_b64_v4i16((LAS s16x4*)(q + 16 * BN * 2));
      fb[st][j] = __builtin_bit_cast(bf16x8, __builtin_shufflevector(u, v, 0, 1, 2, 3, 4, 5, 6, 7));
      if (j == 0) {
#pragma unroll
        for (int i = 0; i < TM; i++)
          fa[st][i] = MODE == 1 ? *(const LAS bf16x8*)(s + oa[MODE == 1 ? st : 0] + i * 2048)
                                : *(const LAS bf16x8*)(s + oa[0] + i * 2048 + st * 1024);
      }
    }
  };
  const bf16x8 ones = {1.f, 1.f, 1.f, 1.f, 1.f, 1.f, 1.f, 1.f};
  auto mm = [&](int st) {
#pragma unroll
    for (int j = 0; j < TN; j++) {
#pragma unroll
      for (int i = 0; i < TM; i++) acc[i][j] = __builtin_amdgcn_mfma_f32_16x16x32_bf16(fb[st][j], fa[st][i], acc[i][j], 0, 0, 0);
    }
    if (MODE == 1) {
      cs = __builtin_amdgcn_mfma_f32_16x16x32_bf16(fb[st][0], ones, cs, 0, 0, 0);
#pragma unroll
      for (int i = 0; i < 2; i++) {
        sx[i] = __builtin_amdgcn_mfma_f32_16x16x32_bf16(ones, fa[st][i], sx[i], 0, 0, 0);
        sq[i] = __builtin_amdgcn_mfma_f32_16x16x32_bf16(fa[st][i], fa[st][i], sq[i], 0, 0, 0);
      }
    }
  };
  auto mix = [&]() {
    constexpr int NR = TM + 2 * TN, NM = TM * TN + (MODE == 1 ? 5 : 0), X2 = NR > NM - 1 ? NR - (NM - 1) : 0, NG = NR - X2;
#pragma unroll
    for (int i = 0; i < NG; i++) {
      __builtin_amdgcn_sched_group_barrier(0x008, 1, 0);
      if (i < X2) __builtin_amdgcn_sched_group_barrier(0x100, 2, 0);
      else __builtin_amdgcn_sched_group_barrier(0x100, 1, 0);
    }
    __builtin_amdgcn_sched_group_barrier(0x008, NM - NG, 0);
    __builtin_amdgcn_sched_barrier(0);
  };
  ST(1);
  bar();
  ST(2);
  rd(0, 0); rd(1, 0);
  __builtin_amdgcn_sched_barrier(0);
  mm(0);
  unsigned base = SB;
  for (int t = 1; t < NT; t++) {
    __builtin_amdgcn_sched_barrier(0);
    wait_lgkm0();
    bar();
    __builtin_amdgcn_sched_barrier(0);
    rd(0, base); mm(1); mix();
    rd(1, base); mm(0); mix();
    base = base == (NS - 1) * SB ? 0 : base + SB;
  }
  mm(1);
  ST(3);

  const __amdgpu_buffer_rsrc_t rws = mk_rsrc(ws, (unsigned)WS_FL);
  auto st16 = [&](unsigned off, u32x4 v) { __builtin_amdgcn_raw_buffer_store_b128(v, rws, off, 0, STAUX); };
  auto st8 = [&](unsigned off, u32x2 v) { __builtin_amdgcn_raw_buffer_store_b64(v, rws, off, 0, STAUX); };
  if (MODE == 1 || MODE == 3) {
    const int q = tn >> 2, sh = (q & 1) ? -1 - (q >> 1) : 1 + (q >> 1);
    if (MODE == 1) {
      if (lt == 0) *(f32x4*)(csl + w * 16 + g * 4) = cs;
      wait_lgkm0();
      bar();
    }
#pragma unroll
    for (int i = 0; i < TM; i++) {
      float a = s1[i], b = s2[i];
      if (MODE == 1) {
        a = sx[i & 1][0];
        const f32x4 d = sq[i & 1];
        b = __shfl((lt & 2) ? ((lt & 1) ? d[3] : d[2]) : ((lt & 1) ? d[1] : d[0]), (lt >> 2) * 16 + lt, 64);
      } else { a += xor16(a); b += xor16(b); a += xor32(a); b += xor32(b); }
      const float mean = a * (1.f / D_), var = fmaxf(b * (1.f / D_) - mean * mean, 0.f);
      const float rs = __builtin_amdgcn_rsqf(var + 1e-5f), nm = -mean * rs;
      unsigned pk[TN][2];
#pragma unroll
      for (int j = 0; j < TN; j++) {
        const f32x4 c = *(const f32x4*)(csl + (MODE == 1 ? (j ^ w) * 16 : wn * 112 + j * 16) + g * 4);
        const f32x4 y = acc[i][j] * rs + c * nm;
        const f32x2 u = gelu2(f32x2{y[0], y[1]}), v = gelu2(f32x2{y[2], y[3]});
        pk[j][0] = pk2(u.x, u.y); pk[j][1] = pk2(v.x, v.y);
      }
      if (MODE == 1) {
        const int td = (tg0 + i) * 16 + lt + sh, tw = td & (S_ - 1);
        const unsigned keep = td == tw ? 0xffffffffu : 0u;
#pragma unroll
        for (int j = 0; j < TN; j += 2) {
          const bool sw = w & 1;
          const u32x4 v = {(sw ? pk[j + 1][0] : pk[j][0]) & keep, (sw ? pk[j + 1][1] : pk[j][1]) & keep,
                           (sw ? pk[j][0] : pk[j + 1][0]) & keep, (sw ? pk[j][1] : pk[j + 1][1]) & keep};
          st16(WS_G + fo_block<D_>(tw >> 4, (n0 >> 5) + ((j ^ w) >> 1)) + (g * 16 + (tw & 15)) * 16, v);
        }
      } else {
        const int kb0 = tn * 7 + wn * 4;
        if (wn == 0) {
#pragma unroll
          for (int j = 0; j < 3; j++)
            st16(WS_H2 + fo_block<F_>(tg0 + i, kb0 + j) + l * 16, u32x4{pk[2 * j][0], pk[2 * j][1], pk[2 * j + 1][0], pk[2 * j + 1][1]});
          st8(WS_H2 + fo_block<F_>(tg0 + i, kb0 + 3) + l * 16, u32x2{pk[TN - 1][0], pk[TN - 1][1]});
        } else {
          st8(WS_H2 + fo_block<F_>(tg0 + i, kb0 - 1) + l * 16 + 8, u32x2{pk[0][0], pk[0][1]});
#pragma unroll
          for (int j = 0; j < 3; j++)
            st16(WS_H2 + fo_block<F_>(tg0 + i, kb0 + j) + l * 16, u32x4{pk[(2 * j + 1) % TN][0], pk[(2 * j + 1) % TN][1], pk[(2 * j + 2) % TN][0], pk[(2 * j + 2) % TN][1]});
        }
      }
    }
  }
  if (MODE == 2) {
#pragma unroll
    for (int i = 0; i < TM; i++) {
      unsigned pk[TN][2];
      float a = 0.f, b = 0.f;
#pragma unroll
      for (int j = 0; j < TN; j++)
#pragma unroll
        for (int e = 0; e < 2; e++) {
          const unsigned h = pk2(acc[i][j][2 * e], acc[i][j][2 * e + 1]), x = xr[MODE == 2 ? i : 0][MODE == 2 ? j : 0][e];
          const unsigned y = pk2(lo16(x) + lo16(h), hi16(x) + hi16(h));
          pk[j][e] = y;
          a += lo16(y) + hi16(y); b += lo16(y) * lo16(y) + hi16(y) * hi16(y);
        }
#pragma unroll
      for (int j = 0; j < TN; j += 2)
        st16(WS_X1 + fo_block<D_>(tg0 + i, (n0 >> 5) + (j >> 1)) + l * 16, u32x4{pk[j][0], pk[j][1], pk[j + 1][0], pk[j + 1][1]});
      a += xor16(a); b += xor16(b); a += xor32(a); b += xor32(b);
      if (g == 0) st8(WS_ST + ((tg0 + i) * 16 + lt) * 128 + tn * 8, __builtin_bit_cast(u32x2, f32x2{a, b}));
    }
  }
  if (MODE == 4) {
    const __amdgpu_buffer_rsrc_t rx = mk_rsrc(ws + WS_EX, 256 * 32768);
    f32x4 mine[2][TN];
#pragma unroll
    for (int i = 0; i < 2; i++)
#pragma unroll
      for (int j = 0; j < TN; j++) {
        const f32x4 snd = ks ? acc[i][j] : acc[(2 + i) % TM][j];
        mine[i][j] = ks ? acc[(2 + i) % TM][j] : acc[i][j];
        __builtin_amdgcn_raw_buffer_store_b128(__builtin_bit_cast(u32x4, snd), rx, bid * 32768 + w * 8192 + (i * 4 + j) * 1024 + l * 16, 0, 16);
      }
    asm volatile("s_waitcnt vmcnt(0)" ::: "memory");
    bar();
    if (w == 0) {
      if (l == 0) __hip_atomic_store((GAS unsigned*)(ws + WS_FL + bid * 128), 1u, __ATOMIC_RELAXED, __HIP_MEMORY_SCOPE_AGENT);
      while (__hip_atomic_load((GAS unsigned*)(ws + WS_FL + (bid ^ 1) * 128), __ATOMIC_RELAXED, __HIP_MEMORY_SCOPE_AGENT) != 1u)
        __builtin_amdgcn_s_sleep(1);
    }
    bar();
    ST(4);
    u32x4 oth[2][TN];
#pragma unroll
    for (int i = 0; i < 2; i++)
#pragma unroll
      for (int j = 0; j < TN; j++)
        oth[i][j] = __builtin_amdgcn_raw_buffer_load_b128(rx, (bid ^ 1) * 32768 + w * 8192 + (i * 4 + j) * 1024 + l * 16, 0, 16);
#pragma unroll
    for (int i = 0; i < 2; i++)
#pragma unroll
      for (int jp = 0; jp < 2; jp++) {
        unsigned y[4];
#pragma unroll
        for (int e = 0; e < 4; e++) {
          const int j = (jp * 2 + (e >> 1)) % TN, r = (e & 1) * 2;
          const f32x4 o = __builtin_bit_cast(f32x4, oth[i][j]);
          const unsigned h = pk2(mine[i][j][r] + o[r], mine[i][j][r + 1] + o[r + 1]), x = x1r[MODE == 4 ? i : 0][MODE == 4 ? jp : 0][e];
          y[e] = pk2(lo16(x) + lo16(h), hi16(x) + hi16(h));
        }
        const u32x2 p0 = __builtin_amdgcn_permlane16_swap(y[0], y[2], false, false);
        const u32x2 p1 = __builtin_amdgcn_permlane16_swap(y[1], y[3], false, false);
        const int col = n0 + jp * 32 + (g & 1) * 16 + (g >> 1) * 8;
        *(GAS u32x4*)(outp + ((size_t)((tg0 + ks * 2 + i) * 16 + lt) * D_ + col) * 2) = u32x4{p0[0], p1[0], p0[1], p1[1]};
      }
  }
  ST(5);
#endif
}

template <int MODE> static void launch(const void* A, const void* W, const void* X, const void* W3, const void* Pf, void* ws, void* out, hipStream_t st) {
  constexpr int lds = Cfg<MODE>::NS * (16384 + Cfg<MODE>::BN * 128) + 1024;
  (void)hipFuncSetAttribute((const void*)gemm_k<MODE>, hipFuncAttributeMaxDynamicSharedMemorySize, lds);
  gemm_k<MODE><<<256, 512, lds, st>>>((const char*)A, (const char*)W, (const char*)X, (const char*)W3, (const char*)Pf, (char*)ws, (char*)out);
}

extern "C" void kernel_launch(void* const* d_in, const int* in_sizes, int n_in, void* d_out, int out_size, void* d_ws,
                              size_t ws_size, hipStream_t stream) {
  char* ws = (char*)d_ws;
  launch<1>(d_in[0], d_in[1], d_in[0], d_in[3], d_in[2], ws, d_out, stream);
  launch<2>(ws + WS_G, d_in[2], d_in[0], d_in[3], d_in[4], ws, d_out, stream);
  launch<3>(ws + WS_X1, d_in[3], d_in[0], d_in[3], d_in[4], ws, d_out, stream);
  launch<4>(ws + WS_H2, d_in[4], d_in[0], d_in[3], d_in[4], ws, d_out, stream);
}
```

```cpp
#include <hip/hip_runtime.h>
#include <hip/hip_bf16.h>
#include <type_traits>

typedef __attribute__((ext_vector_type(8))) __bf16 bf16x8;
typedef __attribute__((ext_vector_type(4))) __bf16 bf16x4;
typedef __attribute__((ext_vector_type(2))) __bf16 bf16x2;
typedef __attribute__((ext_vector_type(4))) short s16x4;
typedef __attribute__((ext_vector_type(8))) short s16x8;
typedef __attribute__((ext_vector_type(4))) float f32x4;
typedef __attribute__((ext_vector_type(2))) float f32x2;
typedef __attribute__((ext_vector_type(4))) unsigned u32x4;
typedef __attribute__((ext_vector_type(2))) unsigned u32x2;
#define LAS __attribute__((address_space(3)))
#define GAS __attribute__((address_space(1)))
#define DEVI __device__ __forceinline__

constexpr int S_ = 2048, D_ = 1024, F_ = 3584;
constexpr size_t WS_G = 0;
constexpr size_t WS_X1 = WS_G + (size_t)S_ * D_ * 2;
constexpr size_t WS_H2 = WS_X1 + (size_t)S_ * D_ * 2;
constexpr size_t WS_ST = WS_H2 + (size_t)S_ * F_ * 2;
constexpr size_t WS_CS = WS_ST + (size_t)S_ * 16 * 8;
constexpr size_t WS_EX = WS_CS + (size_t)16 * F_ * 4;
constexpr size_t WS_FL = WS_EX + (size_t)256 * 32768;

#define ST(i)
#define LDAUX 16
#define LSL 1
#define FSP 4352
#define FST 256
#define PSL 1
#define SIDE1 16
#define SIDE0 8
#define TAILM 2
typedef unsigned long long u64;
template <int MODE> struct Cfg;
template <> struct Cfg<1> { static constexpr int BN = 64, NT = 16, LDW = 1024, TM = 2, TN = 4, KD = 1024, S0 = 0; };
template <> struct Cfg<2> { static constexpr int BN = 64, NT = 16, LDW = 1024, TM = 2, TN = 4, KD = 1024, S0 = 16; };
template <> struct Cfg<3> { static constexpr int BN = 224, NT = 16, LDW = 3584, TM = 4, TN = 7, KD = 1024, S0 = 32; };
template <> struct Cfg<4> { static constexpr int BN = 128, NT = 28, LDW = 1024, TM = 4, TN = 4, KD = 3584, S0 = 48; };
constexpr int NSTG = 76, CTL = 156 * 1024, C_LAND = CTL, C_CONS = CTL + 16, C_CNT = CTL + 32, C_CS1 = CTL + 256, C_CS3 = CTL + 512,
              C_DUM = CTL + 1536, C_RS = CTL + 2048, LDS_BYTES = 160 * 1024;
constexpr int ph_of(int s) { return s < 16 ? 1 : s < 32 ? 2 : s < 48 ? 3 : 4; }
constexpr int ph_first(int p) { return p == 1 ? 0 : p == 2 ? 16 : p == 3 ? 32 : p == 4 ? 48 : NSTG; }
constexpr int st_nlb(int s) { return s < 32 ? 2 : s < 48 ? 7 : 4; }
constexpr int st_size(int s) { return 16384 + st_nlb(s) * 4096; }
constexpr int st_off(int s) {
  return 1024 * (s < 32 ? (s % 6) * 24 : s < 48 ? ((s - 32) % 3 == 0 ? 48 : (s - 32) % 3 == 1 ? 92 : 4)
                                               : ((s & 3) == 0 ? 92 : (s & 3) == 1 ? 124 : (s & 3) == 2 ? 16 : 48));
}
constexpr int st_need(int s) {
  int n = 0;
  for (int v = 0; v < s; v++)
    if (st_off(v) < st_off(s) + st_size(s) && st_off(s) < st_off(v) + st_size(v)) n = v + 1;
  return n;
}
enum { O_WAITC, O_W, O_A, O_POLL, O_LAND, O_SIDE, O_FIN };
struct Prog { int n, kind[400], s[400], c[400]; };
constexpr Prog mk_prog() {
  Prog P{};
  int own[700] = {}, nl = 0, nW = 0, nA = 0, cons = 0;
  auto op = [&](int k, int s, int c) { P.kind[P.n] = k; P.s[P.n] = s; P.c[P.n] = c; P.n++; };
  auto ld = [&](int o, int k) { for (int i = 0; i < k; i++) own[nl++] = o; };
  auto free_ = [&](int s) {};
  auto iw = [&]() { free_(nW); op(O_W, nW, 0); ld(nW, st_nlb(nW)); nW++; };
  auto ia = [&]() { free_(nA); op(O_A, nA, 0); ld(nA, 4); if (nW == nA) iw(); nA++; };
  for (int u = 0; u < NSTG; u++) {
    const int p = ph_of(u), e = ph_first(p + 1);
    if (u == ph_first(p)) {
      if (p == 2) op(O_FIN, 1, 0);
      if (p > 1) op(O_POLL, p, 0);
      ia(); ia();
      if (p == 3) { op(O_SIDE, 3, 0); ld(-1, 8); }
    }
    int last = 0;
    for (int i = 0; i < nl; i++) if (own[i] == u) last = i;
    if (u == 34) op(O_FIN, 3, 0);
    op(O_LAND, u, nl - 1 - last);
    for (int k = 0; k < 2; k++) if (nA < e && st_need(nA) <= u) {
      ia();
      if (nA == SIDE1) { op(O_SIDE, 1, 0); ld(-1, 8); }
      if (nA == SIDE0) { op(O_SIDE, 4, 0); ld(-1, 2); }
      if (nA == 32) { op(O_SIDE, 2, 0); ld(-1, 7); }
    }
    if (nA == e) { while (nW < NSTG && nW < ph_first(p + 2) && st_need(nW) <= u) iw(); }
  }
  return P;
}
constexpr Prog PROG = mk_prog();
#if defined(__HIP_DEVICE_COMPILE__)
DEVI __amdgpu_buffer_rsrc_t mk_rsrc(const void* p, unsigned bytes) {
  return __builtin_amdgcn_make_buffer_rsrc((void*)p, 0, bytes, 0x00020000);
}
template <int N> DEVI void wait_vm() { __builtin_amdgcn_s_waitcnt(0x0F70 | (N & 15) | ((N >> 4) << 14)); }
DEVI void wait_lgkm0() { __builtin_amdgcn_s_waitcnt(0xC07F); }
DEVI void bar() { asm volatile("s_barrier" ::: "memory"); }
DEVI unsigned pk2(float a, float b) {
  bf16x2 v = {(__bf16)a, (__bf16)b};
  return __builtin_bit_cast(unsigned, v);
}
DEVI float lo16(unsigned u) { return __builtin_bit_cast(float, u << 16); }
DEVI float hi16(unsigned u) { return __builtin_bit_cast(float, u & 0xffff0000u); }
DEVI float xor16(float v) { return __shfl_xor(v, 16, 64); }
DEVI float xor32(float v) { return __shfl_xor(v, 32, 64); }
DEVI f32x2 gelu2(f32x2 x) {
  const float c0 = -2.0f * 0.7978845608028654f * 1.4426950408889634f, c1 = c0 * 0.044715f;
  f32x2 z = x * (x * x * c1 + c0);
  f32x2 e = {__builtin_amdgcn_exp2f(z.x), __builtin_amdgcn_exp2f(z.y)};
  e = e + 1.0f;
  f32x2 r = {__builtin_amdgcn_rcpf(e.x), __builtin_amdgcn_rcpf(e.y)};
  return x * r;
}
template <int I, int N, class F> DEVI void sfor(F&& f) {
  if constexpr (I < N) { f(std::integral_constant<int, I>{}); sfor<I + 1, N>(f); }
}
template <int BN> DEVI int swz(int rho) {
  return BN == 64 ? 2 * ((rho >> 1) & 3) : BN == 128 ? 2 * (rho & 7) : 2 * ((rho >> 2) & 1);
}
template <int KD> DEVI unsigned fo_block(int tg, int kb) {
  return (unsigned)((((tg >> 3) * (KD / 64) + (kb >> 1)) * 16 + (tg & 7) * 2 + (kb & 1)) * 1024);
}

DEVI void tile_of(int mode, int bid, int& tm, int& tn, int& ks) {
  const int xcd = bid & 7, loc = bid >> 3;
  ks = 0;
  if (mode <= 2) { tm = (xcd >> 1) * 4 + (loc >> 3); tn = (xcd & 1) * 8 + (loc & 7); }
  else if (mode == 3) { tm = (xcd >> 2) * 8 + (loc >> 2); tn = (xcd & 3) * 4 + (loc & 3); }
  else { tm = (xcd >> 1) * 4 + (loc >> 3); tn = loc & 7; ks = xcd & 1; }
}
#define SPIN (1 << 20)
DEVI unsigned lds_min4(const char* sm, int off) {
  const u32x4 v = *(const volatile LAS u32x4*)((const LAS char*)sm + off);
  return __builtin_amdgcn_readfirstlane(min(min(v[0], v[1]), min(v[2], v[3])));
}
template <int SL> DEVI void lds_wait4(const char* sm, int off, unsigned n) {
#pragma nounroll
  for (int i = 0; i < SPIN && lds_min4(sm, off) < n; i++) { if (SL) __builtin_amdgcn_s_sleep(SL); }
  asm volatile("" ::: "memory");
}
DEVI void lds_set(char* sm, int off, unsigned v) { *(volatile LAS unsigned*)((LAS char*)sm + off) = v; }
DEVI unsigned lds_arrive(char* sm, int off) {
  unsigned r = 0;
  if ((threadIdx.x & 63) == 0) r = __hip_atomic_fetch_add((LAS unsigned*)((LAS char*)sm + off), 1u, __ATOMIC_RELAXED, __HIP_MEMORY_SCOPE_WORKGROUP);
  return __builtin_amdgcn_readfirstlane(r);
}
DEVI GAS u64* flag(char* ws, int k, int i) { return (GAS u64*)(ws + WS_FL + 4096 + (k * 64 + (i >> 4)) * FSP + (i & 15) * FST); }
DEVI void poll(GAS u64* p, u64 tag) {
#pragma nounroll
  for (int i = 0; i < SPIN; i++) {
    if (__all(__hip_atomic_load(p, __ATOMIC_RELAXED, __HIP_MEMORY_SCOPE_AGENT) == tag)) break;
    __builtin_amdgcn_s_sleep(PSL);
  }
  asm volatile("" ::: "memory");
}
DEVI bool signal(char* sm, int cnt, GAS u64* f, u64 tag) {
  asm volatile("s_waitcnt vmcnt(0)" ::: "memory");
  const bool last = lds_arrive(sm, C_CNT + cnt * 4) == 3;
  if (last && (threadIdx.x & 63) == 0) __hip_atomic_store(f, tag, __ATOMIC_RELAXED, __HIP_MEMORY_SCOPE_AGENT);
  return last;
}

struct Ptrs { const char *x, *w1, *w2, *w3, *w4; char *ws, *out; };

template <int MODE> DEVI unsigned w_voff(int i, int lw, int l, int tn, int ks) {
  using C = Cfg<MODE>;
  constexpr int BN = C::BN;
  const int c = (lw * (BN / 32) + i) * 64 + l, r = c / (BN / 8), p = c % (BN / 8);
  const int k = MODE == 1 ? (r & 32) + 8 * ((r >> 2) & 3) + 4 * ((r >> 4) & 1) + (r & 3) : r;
  return (unsigned)(((ks * C::NT * 64 + k) * C::LDW + tn * BN) * 2 + ((p ^ swz<BN>(r)) << 4));
}

DEVI void loader(const Ptrs& P, char* sm, u64 tag) {
  char* ws = P.ws;
  const int bid = blockIdx.x, l = threadIdx.x & 63, lw = __builtin_amdgcn_readfirstlane(threadIdx.x >> 6) - 4;
  int tm1, tn1, tm3, tn3, tm4, tn4, ks4, z;
  tile_of(1, bid, tm1, tn1, z); tile_of(3, bid, tm3, tn3, z); tile_of(4, bid, tm4, tn4, ks4);
  const __amdgpu_buffer_rsrc_t rx = mk_rsrc(P.x, S_ * D_ * 2), rws = mk_rsrc(ws, (unsigned)WS_FL);
  const __amdgpu_buffer_rsrc_t r1 = mk_rsrc(P.w1, D_ * D_ * 2), r2 = mk_rsrc(P.w2, D_ * D_ * 2), r3 = mk_rsrc(P.w3, D_ * F_ * 2), r4 = mk_rsrc(P.w4, D_ * F_ * 2);
  unsigned vx[4], v1[2], v2[2], v3[7], v4[4];
#pragma unroll
  for (int i = 0; i < 4; i++) { const int c = (lw * 4 + i) * 64 + l, r = c >> 3; vx[i] = (unsigned)((tm1 * 128 + r) * D_ * 2 + (((c & 7) ^ (r & 7)) << 4)); }
#pragma unroll
  for (int i = 0; i < 2; i++) { v1[i] = w_voff<1>(i, lw, l, tn1, 0); v2[i] = w_voff<2>(i, lw, l, tn1, 0); }
  const unsigned va = lw * 4096 + l * 16;
  const int rot = ((tn1 & 7) + 2 * (tm1 & 3)) & 15;
  const int rg = l / 7, w3col = tn1 * 224 + lw * 56 + (l - rg * 7) * 8;
  u32x4 pf[8], pg[2] = {};
  const int aland = l == 0 ? C_LAND + lw * 4 : C_DUM + l * 4;
  sfor<0, PROG.n>([&](auto ic) {
    constexpr int K = PROG.kind[ic.value], s = PROG.s[ic.value], c = PROG.c[ic.value];
    constexpr int MODE = (K == O_POLL || K == O_SIDE || K == O_FIN) ? s : ph_of(s);
    LAS char* base = (LAS char*)sm + st_off(s);
    if constexpr (K == O_WAITC) lds_wait4<LSL>(sm, C_CONS, c);
    if constexpr (K == O_W) {
      if constexpr (s == 32) {
#pragma unroll
        for (int i = 0; i < 7; i++) v3[i] = w_voff<3>(i, lw, l, tn3, 0);
      }
      if constexpr (s == 48) {
#pragma unroll
        for (int i = 0; i < 4; i++) v4[i] = w_voff<4>(i, lw, l, tn4, ks4);
      }
      constexpr int NLB = st_nlb(s), t = s - ph_first(MODE);
      const unsigned so = (MODE <= 2 ? ((t + rot) & 15) : t) * (64 * Cfg<MODE>::LDW * 2);
#pragma unroll
      for (int i = 0; i < NLB; i++)
        __builtin_amdgcn_raw_ptr_buffer_load_lds(MODE == 1 ? r1 : MODE == 2 ? r2 : MODE == 3 ? r3 : r4, base + 16384 + (lw * NLB + i) * 1024, 16,
                                                 MODE == 1 ? v1[i & 1] : MODE == 2 ? v2[i & 1] : MODE == 3 ? v3[i] : v4[i & 3], so, 0, 0);
    }
    if constexpr (K == O_A) {
      constexpr int t = s - ph_first(MODE);
      const unsigned so = MODE == 1   ? ((t + rot) & 15) * 128
                          : MODE == 2 ? (unsigned)WS_G + (tm1 * 16 + ((t + rot) & 15)) * 16384
                          : MODE == 3 ? (unsigned)WS_X1 + (tm3 * 16 + t) * 16384
                                      : (unsigned)WS_H2 + (tm4 * 56 + ks4 * 28 + t) * 16384;
      sfor<0, 4>([&](auto jc) {
        constexpr int i = jc.value;
        if constexpr (MODE == 1) __builtin_amdgcn_raw_ptr_buffer_load_lds(rx, base + (lw * 4 + i) * 1024, 16, vx[i], so, 0, 0);
        else __builtin_amdgcn_raw_ptr_buffer_load_lds(rws, base + lw * 4096, 16, va, so, i * 1024, LDAUX);
      });
    }
    if constexpr (K == O_POLL) {
      ST(0);
      bar();
      ST(1);
    }
    if constexpr (K == O_LAND) {
      wait_vm<c>();
      asm volatile("" ::: "memory");
      bar();
    }
    if constexpr (K == O_SIDE) {
      __builtin_amdgcn_sched_barrier(0);
      if constexpr (MODE == 1) {
#pragma unroll
        for (int r = 0; r < 8; r++) pf[r] = __builtin_amdgcn_raw_buffer_load_b128(r3, ((tm1 * 64 + (rg & 7) * 8 + r) * F_ + w3col) * 2, 0, 0);
      }
      if constexpr (MODE == 4) {
#pragma unroll
        for (int r = 0; r < 2; r++) pg[r] = __builtin_amdgcn_raw_buffer_load_b128(r2, bid * 8192 + r * 4096 + lw * 1024 + l * 16, 0, 0);
      }
      if constexpr (MODE == 2) {
#pragma unroll
        for (int r = 0; r < 7; r++) pf[r] = __builtin_amdgcn_raw_buffer_load_b128(r4, bid * 28672 + r * 4096 + va / 4096 * 1024 + l * 16, 0, 0);
      }
      if constexpr (MODE == 3) {
#pragma unroll
        for (int r = 0; r < 7; r++) asm volatile("" ::"v"(pf[r]));
#pragma unroll
        for (int r = 0; r < 4; r++)
          pf[r] = __builtin_amdgcn_raw_buffer_load_b128(rws, WS_CS + ((((l / 14) & 3) * 4 + r) * F_ + tn3 * 224 + lw * 56 + (l % 14) * 4) * 4, 0, 16);
#pragma unroll
        for (int r = 0; r < 4; r++) pf[4 + r] = __builtin_amdgcn_raw_buffer_load_b128(rws, WS_ST + (tm3 * 128 + lw * 32) * 128 + r * 1024 + l * 16, 0, 16);
      }
      asm volatile("" ::: "memory");
    }
    if constexpr (K == O_FIN && MODE == 1) {
      asm volatile("" ::"v"(pg[0]), "v"(pg[1]));
#pragma unroll
      for (int r = 0; r < 8; r++) asm volatile("" : "+v"(pf[r]));
      float a[8] = {0.f, 0.f, 0.f, 0.f, 0.f, 0.f, 0.f, 0.f};
#pragma unroll
      for (int r = 0; r < 8; r++)
#pragma unroll
        for (int e = 0; e < 4; e++) { a[2 * e] += lo16(pf[r][e]); a[2 * e + 1] += hi16(pf[r][e]); }
#pragma unroll
      for (int e = 0; e < 8; e++) {
        a[e] += __shfl_down(a[e], 28, 64); a[e] += __shfl_down(a[e], 14, 64); a[e] += __shfl_down(a[e], 7, 64);
      }
      if (l < 7) {
        __builtin_amdgcn_raw_buffer_store_b128(__builtin_bit_cast(u32x4, f32x4{a[0], a[1], a[2], a[3]}), rws, WS_CS + (tm1 * F_ + w3col) * 4, 0, 16);
        __builtin_amdgcn_raw_buffer_store_b128(__builtin_bit_cast(u32x4, f32x4{a[4], a[5], a[6], a[7]}), rws, WS_CS + (tm1 * F_ + w3col) * 4 + 16, 0, 16);
      }
      signal(sm, 4, flag(ws, 3, tn1 * 16 + tm1), tag);
    }
    if constexpr (K == O_FIN && MODE == 3) {
#pragma unroll
      for (int r = 0; r < 8; r++) asm volatile("" : "+v"(pf[r]));
      f32x4 v = (__builtin_bit_cast(f32x4, pf[0]) + __builtin_bit_cast(f32x4, pf[1])) + (__builtin_bit_cast(f32x4, pf[2]) + __builtin_bit_cast(f32x4, pf[3]));
#pragma unroll
      for (int e = 0; e < 4; e++) { v[e] += __shfl_down(v[e], 28, 64); v[e] += __shfl_down(v[e], 14, 64); }
      if (l < 14) *(LAS f32x4*)((LAS char*)sm + C_CS3 + (lw * 56 + l * 4) * 4) = v;
#pragma unroll
      for (int r = 0; r < 4; r++) {
        const f32x4 u = __builtin_bit_cast(f32x4, pf[4 + r]);
        float a = u[0] + u[2], b = u[1] + u[3];
#pragma unroll
        for (int m = 1; m < 8; m *= 2) { a += __shfl_xor(a, m, 64); b += __shfl_xor(b, m, 64); }
        const float mean = a * (1.f / D_), var = fmaxf(b * (1.f / D_) - mean * mean, 0.f);
        const float rs = __builtin_amdgcn_rsqf(var + 1e-5f);
        if ((l & 7) == 0) *(LAS f32x2*)((LAS char*)sm + C_RS + (lw * 32 + r * 8 + (l >> 3)) * 8) = f32x2{rs, -mean * rs};
      }
    }
  });
  if (bid == 0 && lw == 0) {
#pragma unroll
    for (int i = 0; i < 4; i++) poll(flag(ws, 0, i * 64 + l), tag);
    if (l == 0) __hip_atomic_store((GAS u64*)(ws + WS_FL), tag, __ATOMIC_RELAXED, __HIP_MEMORY_SCOPE_AGENT);
  }
}

template <int MODE> DEVI void compute(const Ptrs& P, char* sm, u64 tag) {
  using C = Cfg<MODE>;
  constexpr int BN = C::BN, NT = C::NT, TM = C::TM, TN = C::TN, S0 = C::S0;
  char* ws = P.ws;
  const int bid = blockIdx.x, l = threadIdx.x & 63, w = __builtin_amdgcn_readfirstlane(threadIdx.x >> 6), g = l >> 4, lt = l & 15;
  int tm, tn, ks;
  tile_of(MODE, bid, tm, tn, ks);
  const int wm = MODE <= 2 ? w : (w >> 1), wn = MODE <= 2 ? 0 : (w & 1);
  const int tg0 = tm * 8 + wm * TM;
  const int n0 = tn * BN + wn * TN * 16;
  const __amdgpu_buffer_rsrc_t rws = mk_rsrc(ws, (unsigned)WS_FL);
  const int acons = l == 0 ? C_CONS + w * 4 : C_DUM + l * 4;

  f32x4 acc[TM][TN];
#pragma unroll
  for (int i = 0; i < TM; i++)
#pragma unroll
    for (int j = 0; j < TN; j++) acc[i][j] = f32x4{0.f, 0.f, 0.f, 0.f};
  f32x4 cs = {0.f, 0.f, 0.f, 0.f}, sx[2], sq[2];
  sx[0] = sx[1] = sq[0] = sq[1] = cs;
  u32x2 xr[MODE == 2 ? TM : 1][MODE == 2 ? TN : 1];
  u32x4 x1r[MODE == 4 ? 2 : 1][MODE == 4 ? 2 : 1];
  if (MODE == 2) {
#pragma unroll
    for (int i = 0; i < TM; i++)
#pragma unroll
      for (int j = 0; j < TN; j++)
        xr[i][j] = *(const GAS u32x2*)(P.x + ((size_t)((tg0 + i) * 16 + lt) * D_ + n0 + j * 16 + g * 4) * 2);
  }
  unsigned oa[MODE == 1 ? 2 : 1], ob[TN];
  if (MODE == 1) {
    oa[0] = (wm * 32 + lt) * 128 + ((g ^ (lt & 7)) << 4);
    oa[MODE == 1 ? 1 : 0] = (wm * 32 + lt) * 128 + (((4 + g) ^ (lt & 7)) << 4);
  } else oa[0] = wm * TM * 2048 + l * 16;
  {
    const int rho = 4 * g + (lt >> 2), p = l & 3;
#pragma unroll
    for (int j = 0; j < TN; j++)
      ob[j] = (BN == 224 && j >= 2) ? ob[j & 1] + (j >> 1) * 64
                                    : 16384 + rho * BN * 2 + ((((MODE == 1 ? j ^ w : wn * TN + j) * 2) ^ swz<BN>(rho)) + (p >> 1)) * 16 + (p & 1) * 8;
  }
  bf16x8 fa[2][TM], fb[2][TN];
  auto rd = [&](int st, unsigned base) {
    const LAS char* s = (const LAS char*)sm + base;
#pragma unroll
    for (int j = 0; j < TN; j++) {
      const LAS char* q = s + ob[j] + st * 32 * BN * 2;
      const s16x4 u = __builtin_amdgcn_ds_read_tr16_b64_v4i16((LAS s16x4*)q);
      const s16x4 v = __builtin_amdgcn_ds_read_tr16_b64_v4i16((LAS s16x4*)(q + 16 * BN * 2));
      fb[st][j] = __builtin_bit_cast(bf16x8, __builtin_shufflevector(u, v, 0, 1, 2, 3, 4, 5, 6, 7));
      if (j == 0) {
#pragma unroll
        for (int i = 0; i < TM; i++)
          fa[st][i] = MODE == 1 ? *(const LAS bf16x8*)(s + oa[MODE == 1 ? st : 0] + i * 2048)
                                : *(const LAS bf16x8*)(s + oa[0] + i * 2048 + st * 1024);
      }
    }
  };
  const bf16x8 ones = {1.f, 1.f, 1.f, 1.f, 1.f, 1.f, 1.f, 1.f};
  auto mm = [&](int st) {
#pragma unroll
    for (int j = 0; j < TN; j++) {
#pragma unroll
      for (int i = 0; i < TM; i++) acc[i][j] = __builtin_amdgcn_mfma_f32_16x16x32_bf16(fb[st][j], fa[st][i], acc[i][j], 0, 0, 0);
    }
    if (MODE == 1) {
      cs = __builtin_amdgcn_mfma_f32_16x16x32_bf16(fb[st][0], ones, cs, 0, 0, 0);
#pragma unroll
      for (int i = 0; i < 2; i++) {
        sx[i] = __builtin_amdgcn_mfma_f32_16x16x32_bf16(ones, fa[st][i], sx[i], 0, 0, 0);
        sq[i] = __builtin_amdgcn_mfma_f32_16x16x32_bf16(fa[st][i], fa[st][i], sq[i], 0, 0, 0);
      }
    }
  };
  auto mix = [&](auto tl) {
    constexpr int T = tl.value, NR = TM + 2 * TN, NM = TM * TN + (MODE == 1 ? 5 : 0), NK = NM - 1 - (T ? TAILM : 0);
    constexpr int X2 = NR > NK ? NR - NK : 0, NG = NR - X2;
#pragma unroll
    for (int i = 0; i < NG; i++) {
      __builtin_amdgcn_sched_group_barrier(0x008, 1, 0);
      if (i < X2) __builtin_amdgcn_sched_group_barrier(0x100, 2, 0);
      else __builtin_amdgcn_sched_group_barrier(0x100, 1, 0);
    }
    if (T) {
      __builtin_amdgcn_sched_group_barrier(0x200, 1, 0);
      __builtin_amdgcn_sched_group_barrier(0x100, 1, 0);
      __builtin_amdgcn_sched_group_barrier(0x008, NM - NG - 2, 0);
      __builtin_amdgcn_sched_group_barrier(0x002, 3, 0);
      __builtin_amdgcn_sched_group_barrier(0x008, 2, 0);
    } else __builtin_amdgcn_sched_group_barrier(0x008, NM - NG, 0);
    __builtin_amdgcn_sched_barrier(0);
  };
  ST(1);
  if (MODE > 1) bar();
  bar();
  ST(2);
  if (MODE == 4) {
#pragma unroll
    for (int i = 0; i < 2; i++)
#pragma unroll
      for (int j = 0; j < 2; j++)
        x1r[i][j] = __builtin_amdgcn_raw_buffer_load_b128(rws, WS_X1 + fo_block<D_>(tg0 + ks * 2 + i, (n0 >> 5) + j) + l * 16, 0, 16);
  }
  unsigned base = st_off(S0);
  rd(0, base); rd(1, base);
  __builtin_amdgcn_sched_barrier(0);
  mm(0);
  for (int t = S0 + 1; t < S0 + NT; t++) {
    __builtin_amdgcn_sched_barrier(0);
    wait_lgkm0();
    bar();
    base = MODE <= 2 ? (base == 120 * 1024 ? 0 : base + 24 * 1024)
           : MODE == 3 ? (base == 92 * 1024 ? 4 * 1024 : base + 44 * 1024)
                       : (base == 124 * 1024 ? 16 * 1024 : base == 48 * 1024 ? 92 * 1024 : base + 32 * 1024);
    __builtin_amdgcn_sched_barrier(0);
    rd(0, base); mm(1); mix(std::integral_constant<int, 0>{});
    rd(1, base); mm(0); mix(std::integral_constant<int, 0>{});
  }
  mm(1);
  ST(3);

  auto st16 = [&](unsigned off, u32x4 v) { __builtin_amdgcn_raw_buffer_store_b128(v, rws, off, 0, 16); };
  auto st8 = [&](unsigned off, u32x2 v) { __builtin_amdgcn_raw_buffer_store_b64(v, rws, off, 0, 16); };
  if (MODE == 1 || MODE == 3) {
    const int q = tn >> 2, sh = (q & 1) ? -1 - (q >> 1) : 1 + (q >> 1);
    const LAS char* csl = (const LAS char*)sm + (MODE == 1 ? C_CS1 : C_CS3);
    if (MODE == 1) {
      if (lt == 0) *(volatile LAS f32x4*)((LAS char*)sm + C_CS1 + (w * 16 + g * 4) * 4) = cs;
      lds_arrive(sm, C_CNT + 20);
#pragma nounroll
      for (int i = 0; i < SPIN && __builtin_amdgcn_readfirstlane(*(const volatile LAS unsigned*)((LAS char*)sm + C_CNT + 20)) < 4; i++) {}
      asm volatile("" ::: "memory");
    }
#pragma unroll
    for (int i = 0; i < TM; i++) {
      float rs, nm;
      if (MODE == 1) {
        const f32x4 d = sq[i & 1];
        const float a = sx[i & 1][0], b = __shfl((lt & 2) ? ((lt & 1) ? d[3] : d[2]) : ((lt & 1) ? d[1] : d[0]), (lt >> 2) * 16 + lt, 64);
        const float mean = a * (1.f / D_), var = fmaxf(b * (1.f / D_) - mean * mean, 0.f);
        rs = __builtin_amdgcn_rsqf(var + 1e-5f); nm = -mean * rs;
      } else {
        const f32x2 u = *(const LAS f32x2*)((const LAS char*)sm + C_RS + ((wm * TM + i) * 16 + lt) * 8);
        rs = u[0]; nm = u[1];
      }
      unsigned pk[TN][2];
#pragma unroll
      for (int j = 0; j < TN; j++) {
        const f32x4 c = *(const LAS f32x4*)(csl + ((MODE == 1 ? (j ^ w) * 16 : wn * 112 + j * 16) + g * 4) * 4);
        const f32x4 y = acc[i][j] * rs + c * nm;
        const f32x2 u = gelu2(f32x2{y[0], y[1]}), v = gelu2(f32x2{y[2], y[3]});
        pk[j][0] = pk2(u.x, u.y); pk[j][1] = pk2(v.x, v.y);
      }
      if (MODE == 1) {
        const int td = (tg0 + i) * 16 + lt + sh, tw = td & (S_ - 1);
        const unsigned keep = td == tw ? 0xffffffffu : 0u;
#pragma unroll
        for (int j = 0; j < TN; j += 2) {
          const bool sw = w & 1;
          const u32x4 v = {(sw ? pk[j + 1][0] : pk[j][0]) & keep, (sw ? pk[j + 1][1] : pk[j][1]) & keep,
                           (sw ? pk[j][0] : pk[j + 1][0]) & keep, (sw ? pk[j][1] : pk[j + 1][1]) & keep};
          st16(WS_G + fo_block<D_>(tw >> 4, (n0 >> 5) + ((j ^ w) >> 1)) + (g * 16 + (tw & 15)) * 16, v);
        }
      } else {
        const int kb0 = tn * 7 + wn * 4;
        if (wn == 0) {
#pragma unroll
          for (int j = 0; j < 3; j++)
            st16(WS_H2 + fo_block<F_>(tg0 + i, kb0 + j) + l * 16, u32x4{pk[2 * j][0], pk[2 * j][1], pk[2 * j + 1][0], pk[2 * j + 1][1]});
          st8(WS_H2 + fo_block<F_>(tg0 + i, kb0 + 3) + l * 16, u32x2{pk[TN - 1][0], pk[TN - 1][1]});
        } else {
          st8(WS_H2 + fo_block<F_>(tg0 + i, kb0 - 1) + l * 16 + 8, u32x2{pk[0][0], pk[0][1]});
#pragma unroll
          for (int j = 0; j < 3; j++)
            st16(WS_H2 + fo_block<F_>(tg0 + i, kb0 + j) + l * 16, u32x4{pk[(2 * j + 1) % TN][0], pk[(2 * j + 1) % TN][1], pk[(2 * j + 2) % TN][0], pk[(2 * j + 2) % TN][1]});
        }
      }
    }
  }
  if (MODE == 2) {
#pragma unroll
    for (int i = 0; i < TM; i++) {
      unsigned pk[TN][2];
      float a = 0.f, b = 0.f;
#pragma unroll
      for (int j = 0; j < TN; j++)
#pragma unroll
        for (int e = 0; e < 2; e++) {
          const unsigned h = pk2(acc[i][j][2 * e], acc[i][j][2 * e + 1]), x = xr[MODE == 2 ? i : 0][MODE == 2 ? j : 0][e];
          const unsigned y = pk2(lo16(x) + lo16(h), hi16(x) + hi16(h));
          pk[j][e] = y;
          a += lo16(y) + hi16(y); b += lo16(y) * lo16(y) + hi16(y) * hi16(y);
        }
#pragma unroll
      for (int j = 0; j < TN; j += 2)
        st16(WS_X1 + fo_block<D_>(tg0 + i, (n0 >> 5) + (j >> 1)) + l * 16, u32x4{pk[j][0], pk[j][1], pk[j + 1][0], pk[j + 1][1]});
      a += xor16(a); b += xor16(b); a += xor32(a); b += xor32(b);
      if (g == 0) st8(WS_ST + ((tg0 + i) * 16 + lt) * 128 + tn * 8, __builtin_bit_cast(u32x2, f32x2{a, b}));
    }
  }
  if (MODE <= 3) {
    ST(4);
    int um, un, uk;
    tile_of(MODE + 1, bid, um, un, uk);
    GAS u64* f = MODE == 1   ? flag(ws, 0, ((um + 15) * 16 + (l < 48 ? l : 16)) & 255)
                 : MODE == 2 ? (l < 32 ? flag(ws, 1, um * 16 + (l & 15)) : flag(ws, 3, un * 16 + (l & 15)))
                             : flag(ws, 2, um * 16 + uk * 8 + (l & 7));
    if (signal(sm, MODE - 1, flag(ws, MODE - 1, tm * 16 + tn), tag)) poll(f, tag);
  }
  if (MODE == 4) {
    f32x4 mine[2][TN];
#pragma unroll
    for (int i = 0; i < 2; i++)
#pragma unroll
      for (int j = 0; j < TN; j++) {
        st16(WS_EX + bid * 32768 + w * 8192 + (i * 4 + j) * 1024 + l * 16, __builtin_bit_cast(u32x4, ks ? acc[i][j] : acc[(2 + i) % TM][j]));
        mine[i][j] = ks ? acc[(2 + i) % TM][j] : acc[i][j];
      }
    asm volatile("s_waitcnt vmcnt(0)" ::: "memory");
    if (l == 0) __hip_atomic_store(flag(ws, 4, bid * 4 + w), tag, __ATOMIC_RELAXED, __HIP_MEMORY_SCOPE_AGENT);
    poll(flag(ws, 4, (bid ^ 1) * 4 + w), tag);
    ST(4);
    u32x4 oth[2][TN];
#pragma unroll
    for (int i = 0; i < 2; i++)
#pragma unroll
      for (int j = 0; j < TN; j++)
        oth[i][j] = __builtin_amdgcn_raw_buffer_load_b128(rws, WS_EX + (bid ^ 1) * 32768 + w * 8192 + (i * 4 + j) * 1024 + l * 16, 0, 16);
#pragma unroll
    for (int i = 0; i < 2; i++)
#pragma unroll
      for (int jp = 0; jp < 2; jp++) {
        unsigned y[4];
#pragma unroll
        for (int e = 0; e < 4; e++) {
          const int j = (jp * 2 + (e >> 1)) % TN, r = (e & 1) * 2;
          const f32x4 o = __builtin_bit_cast(f32x4, oth[i][j]);
          const unsigned h = pk2(mine[i][j][r] + o[r], mine[i][j][r + 1] + o[r + 1]), x = x1r[MODE == 4 ? i : 0][MODE == 4 ? jp : 0][e];
          y[e] = pk2(lo16(x) + lo16(h), hi16(x) + hi16(h));
        }
        const u32x2 p0 = __builtin_amdgcn_permlane16_swap(y[0], y[2], false, false);
        const u32x2 p1 = __builtin_amdgcn_permlane16_swap(y[1], y[3], false, false);
        const int col = n0 + jp * 32 + (g & 1) * 16 + (g >> 1) * 8;
        *(GAS u32x4*)(P.out + ((size_t)((tg0 + ks * 2 + i) * 16 + lt) * D_ + col) * 2) = u32x4{p0[0], p1[0], p0[1], p1[1]};
      }
  }
  ST(5);
}
#endif

__global__ __launch_bounds__(512, 2) void mixer_k(const char* __restrict__ x, const char* __restrict__ w1, const char* __restrict__ w2,
                                                  const char* __restrict__ w3, const char* __restrict__ w4, char* ws, char* out) {
#if defined(__HIP_DEVICE_COMPILE__)
  extern __shared__ __attribute__((aligned(16))) char sm[];
  constexpr int MODE = 1;
  ST(0);
  if (threadIdx.x < 16) lds_set(sm, CTL + threadIdx.x * 4, 0u);
  const u64 tag = __hip_atomic_load((GAS u64*)(ws + WS_FL), __ATOMIC_RELAXED, __HIP_MEMORY_SCOPE_AGENT) + 1;
  const Ptrs P = {x, w1, w2, w3, w4, ws, out};
  wait_lgkm0();
  bar();
  if (threadIdx.x >= 256) loader(P, sm, tag);
  else { compute<1>(P, sm, tag); compute<2>(P, sm, tag); compute<3>(P, sm, tag); compute<4>(P, sm, tag); }
#endif
}

extern "C" void kernel_launch(void* const* d_in, const int* in_sizes, int n_in, void* d_out, int out_size, void* d_ws,
                              size_t ws_size, hipStream_t stream) {
  (void)hipFuncSetAttribute((const void*)mixer_k, hipFuncAttributeMaxDynamicSharedMemorySize, LDS_BYTES);
  mixer_k<<<256, 512, LDS_BYTES, stream>>>((const char*)d_in[0], (const char*)d_in[1], (const char*)d_in[2], (const char*)d_in[3],
                                           (const char*)d_in[4], (char*)d_ws, (char*)d_out);
}
```

```cpp
#include <hip/hip_runtime.h>
#include <hip/hip_bf16.h>
#include <type_traits>

typedef __attribute__((ext_vector_type(8))) __bf16 bf16x8;
typedef __attribute__((ext_vector_type(4))) __bf16 bf16x4;
typedef __attribute__((ext_vector_type(2))) __bf16 bf16x2;
typedef __attribute__((ext_vector_type(4))) short s16x4;
typedef __attribute__((ext_vector_type(8))) short s16x8;
typedef __attribute__((ext_vector_type(4))) float f32x4;
typedef __attribute__((ext_vector_type(2))) float f32x2;
typedef __attribute__((ext_vector_type(4))) unsigned u32x4;
typedef __attribute__((ext_vector_type(2))) unsigned u32x2;
#define LAS __attribute__((address_space(3)))
#define GAS __attribute__((address_space(1)))
#define DEVI __device__ __forceinline__

constexpr int S_ = 2048, D_ = 1024, F_ = 3584;
constexpr size_t WS_G = 0;
constexpr size_t WS_X1 = WS_G + (size_t)S_ * D_ * 2;
constexpr size_t WS_H2 = WS_X1 + (size_t)S_ * D_ * 2;
constexpr size_t WS_ST = WS_H2 + (size_t)S_ * F_ * 2;
constexpr size_t WS_CS = WS_ST + (size_t)S_ * 16 * 8;
constexpr size_t WS_EX = WS_CS + (size_t)16 * F_ * 4;
constexpr size_t WS_FL = WS_EX + (size_t)256 * 32768;

#define ST(i)
#define LDAUX 16
#define LSL 1
#define FSP 4352
#define FST 256
#define PSL 1
#define SIDE1 16
#define SIDE0 8
#define STAUX 16
#define OAUX 0
#define TCH 15
#define PAIR 2
#define NS4 4
#define SIDE2 32
#define TAILM 2
typedef unsigned long long u64;
template <int MODE> struct Cfg;
template <> struct Cfg<1> { static constexpr int BN = 64, NT = 16, LDW = 1024, TM = 2, TN = 4, KD = 1024, S0 = 0; };
template <> struct Cfg<2> { static constexpr int BN = 64, NT = 16, LDW = 1024, TM = 2, TN = 4, KD = 1024, S0 = 16; };
template <> struct Cfg<3> { static constexpr int BN = 224, NT = 16, LDW = 3584, TM = 4, TN = 7, KD = 1024, S0 = 32; };
template <> struct Cfg<4> { static constexpr int BN = 128, NT = 28, LDW = 1024, TM = 4, TN = 4, KD = 3584, S0 = 48; };
constexpr int NSTG = 76, CTL = 156 * 1024, C_LAND = CTL, C_CONS = CTL + 16, C_CNT = CTL + 32, C_CS1 = CTL + 256, C_CS3 = CTL + 512,
              C_DUM = CTL + 1536, C_RS = CTL + 2048, LDS_BYTES = 160 * 1024;
constexpr int ph_of(int s) { return s < 16 ? 1 : s < 32 ? 2 : s < 48 ? 3 : 4; }
constexpr int ph_first(int p) { return p == 1 ? 0 : p == 2 ? 16 : p == 3 ? 32 : p == 4 ? 48 : NSTG; }
constexpr int st_nlb(int s) { return s < 32 ? 2 : s < 48 ? 7 : 4; }
constexpr int st_size(int s) { return 16384 + st_nlb(s) * 4096; }
constexpr int st_off(int s) {
  return 1024 * (s < 32 ? (s % 6) * 24 : s < 48 ? ((s - 32) % 3 == 0 ? 48 : (s - 32) % 3 == 1 ? 92 : 4)
                                               : NS4 == 5 ? ((s - 48) % 5 == 0 ? 96 : (s - 48) % 5 == 4 ? 128 : ((s - 48) % 5 - 1) * 32)
                                               : ((s & 3) == 0 ? 92 : (s & 3) == 1 ? 124 : (s & 3) == 2 ? 16 : 48));
}
constexpr unsigned ring_seq(int s0, int n, int unit) {
  unsigned q = 0;
  for (int i = 0; i < n; i++) q |= (unsigned)(st_off(s0 + i) / unit) << (5 * i);
  return q;
}
constexpr int st_need(int s) {
  int n = 0;
  for (int v = 0; v < s; v++)
    if (st_off(v) < st_off(s) + st_size(s) && st_off(s) < st_off(v) + st_size(v)) n = v + 1;
  return n;
}
enum { O_WAITC, O_W, O_A, O_POLL, O_LAND, O_SIDE, O_FIN };
struct Prog { int n, kind[400], s[400], c[400]; };
constexpr Prog mk_prog() {
  Prog P{};
  int own[700] = {}, nl = 0, nW = 0, nA = 0, cons = 0;
  auto op = [&](int k, int s, int c) { P.kind[P.n] = k; P.s[P.n] = s; P.c[P.n] = c; P.n++; };
  auto ld = [&](int o, int k) { for (int i = 0; i < k; i++) own[nl++] = o; };
  auto free_ = [&](int s) {};
  auto iw = [&]() { free_(nW); op(O_W, nW, 0); ld(nW, st_nlb(nW)); nW++; };
  auto ia = [&]() { free_(nA); op(O_A, nA, 0); ld(nA, 4); if (nW == nA) iw(); nA++; };
  for (int u = 0; u < NSTG; u++) {
    const int p = ph_of(u), e = ph_first(p + 1);
    if (u == ph_first(p)) {
      if (p == (SIDE1 > 16 ? 3 : 2)) op(O_FIN, 1, 0);
      if (p > 1) op(O_POLL, p, 0);
      ia(); ia();
      if (p == 3) { op(O_SIDE, 3, 0); ld(-1, 8); }
    }
    if (p <= PAIR && (u & 1)) continue;
    int last = 0;
    for (int i = 0; i < nl; i++) if (own[i] == u || (p <= PAIR && own[i] == u + 1)) last = i;
    if (u == 34) op(O_FIN, 3, 0);
    op(O_LAND, u, nl - 1 - last);
    for (int k = 0; k < (p <= PAIR ? 4 : 2); k++) if (nA < e && st_need(nA) <= u) {
      ia();
      if (nA == SIDE1) { op(O_SIDE, 1, 0); ld(-1, 8); }
      if (nA == SIDE0) { op(O_SIDE, 4, 0); ld(-1, 2); }
      if (nA == SIDE2) { op(O_SIDE, 2, 0); ld(-1, 7); }
    }
    if (nA == e) { while (nW < NSTG && nW < ph_first(p + 2) && st_need(nW) <= u) iw(); }
  }
  return P;
}
constexpr Prog PROG = mk_prog();
#if defined(__HIP_DEVICE_COMPILE__)
DEVI __amdgpu_buffer_rsrc_t mk_rsrc(const void* p, unsigned bytes) {
  return __builtin_amdgcn_make_buffer_rsrc((void*)p, 0, bytes, 0x00020000);
}
template <int N> DEVI void wait_vm() { __builtin_amdgcn_s_waitcnt(0x0F70 | (N & 15) | ((N >> 4) << 14)); }
DEVI void wait_lgkm0() { __builtin_amdgcn_s_waitcnt(0xC07F); }
DEVI void bar() { asm volatile("s_barrier" ::: "memory"); }
DEVI unsigned pk2(float a, float b) {
  bf16x2 v = {(__bf16)a, (__bf16)b};
  return __builtin_bit_cast(unsigned, v);
}
DEVI float lo16(unsigned u) { return __builtin_bit_cast(float, u << 16); }
DEVI float hi16(unsigned u) { return __builtin_bit_cast(float, u & 0xffff0000u); }
DEVI float xor16(float v) { return __shfl_xor(v, 16, 64); }
DEVI float xor32(float v) { return __shfl_xor(v, 32, 64); }
DEVI f32x2 gelu2(f32x2 x) {
  const float c0 = -2.0f * 0.7978845608028654f * 1.4426950408889634f, c1 = c0 * 0.044715f;
  f32x2 z = x * (x * x * c1 + c0);
  f32x2 e = {__builtin_amdgcn_exp2f(z.x), __builtin_amdgcn_exp2f(z.y)};
  e = e + 1.0f;
  f32x2 r = {__builtin_amdgcn_rcpf(e.x), __builtin_amdgcn_rcpf(e.y)};
  return x * r;
}
template <int I, int N, class F> DEVI void sfor(F&& f) {
  if constexpr (I < N) { f(std::integral_constant<int, I>{}); sfor<I + 1, N>(f); }
}
template <int BN> DEVI int swz(int rho) {
  return BN == 64 ? 2 * ((rho >> 1) & 3) : BN == 128 ? 2 * (rho & 7) : 2 * ((rho >> 2) & 1);
}
template <int KD> DEVI unsigned fo_block(int tg, int kb) {
  return (unsigned)((((tg >> 3) * (KD / 64) + (kb >> 1)) * 16 + (tg & 7) * 2 + (kb & 1)) * 1024);
}

DEVI void tile_of(int mode, int bid, int& tm, int& tn, int& ks) {
  const int xcd = bid & 7, loc = bid >> 3;
  ks = 0;
  if (mode <= 2) { tm = (xcd >> 1) * 4 + (loc >> 3); tn = (xcd & 1) * 8 + (loc & 7); }
  else if (mode == 3) { tm = (xcd >> 2) * 8 + (loc >> 2); tn = (xcd & 3) * 4 + (loc & 3); }
  else { tm = (xcd >> 1) * 4 + (loc >> 3); tn = loc & 7; ks = xcd & 1; }
}
#define SPIN (1 << 20)
DEVI unsigned lds_min4(const char* sm, int off) {
  const u32x4 v = *(const volatile LAS u32x4*)((const LAS char*)sm + off);
  return __builtin_amdgcn_readfirstlane(min(min(v[0], v[1]), min(v[2], v[3])));
}
template <int SL> DEVI void lds_wait4(const char* sm, int off, unsigned n) {
#pragma nounroll
  for (int i = 0; i < SPIN && lds_min4(sm, off) < n; i++) { if (SL) __builtin_amdgcn_s_sleep(SL); }
  asm volatile("" ::: "memory");
}
DEVI void lds_set(char* sm, int off, unsigned v) { *(volatile LAS unsigned*)((LAS char*)sm + off) = v; }
DEVI unsigned lds_arrive(char* sm, int off) {
  unsigned r = 0;
  if ((threadIdx.x & 63) == 0) r = __hip_atomic_fetch_add((LAS unsigned*)((LAS char*)sm + off), 1u, __ATOMIC_RELAXED, __HIP_MEMORY_SCOPE_WORKGROUP);
  return __builtin_amdgcn_readfirstlane(r);
}
DEVI GAS u64* flag(char* ws, int k, int i) { return (GAS u64*)(ws + WS_FL + 4096 + (k * 64 + (i >> 4)) * FSP + (i & 15) * FST); }
DEVI void poll(GAS u64* p, u64 tag) {
#pragma nounroll
  for (int i = 0; i < SPIN; i++) {
    if (__all(__hip_atomic_load(p, __ATOMIC_RELAXED, __HIP_MEMORY_SCOPE_AGENT) == tag)) break;
    __builtin_amdgcn_s_sleep(PSL);
  }
  asm volatile("" ::: "memory");
}
DEVI bool signal(char* sm, int cnt, GAS u64* f, u64 tag) {
  asm volatile("s_waitcnt vmcnt(0)" ::: "memory");
  const bool last = lds_arrive(sm, C_CNT + cnt * 4) == 3;
  if (last && (threadIdx.x & 63) == 0) __hip_atomic_store(f, tag, __ATOMIC_RELAXED, __HIP_MEMORY_SCOPE_AGENT);
  return last;
}

struct Ptrs { const char *x, *w1, *w2, *w3, *w4; char *ws, *out; };

template <int MODE> DEVI unsigned w_voff(int i, int lw, int l, int tn, int ks) {
  using C = Cfg<MODE>;
  constexpr int BN = C::BN;
  const int c = (lw * (BN / 32) + i) * 64 + l, r = c / (BN / 8), p = c % (BN / 8);
  const int k = MODE == 1 ? (r & 32) + 8 * ((r >> 2) & 3) + 4 * ((r >> 4) & 1) + (r & 3) : r;
  return (unsigned)(((ks * C::NT * 64 + k) * C::LDW + tn * BN) * 2 + ((p ^ swz<BN>(r)) << 4));
}

DEVI void loader(const Ptrs& P, char* sm, u64 tag) {
  char* ws = P.ws;
  const int bid = blockIdx.x, l = threadIdx.x & 63, lw = __builtin_amdgcn_readfirstlane(threadIdx.x >> 6) - 4;
  int tm1, tn1, tm3, tn3, tm4, tn4, ks4, z;
  tile_of(1, bid, tm1, tn1, z); tile_of(3, bid, tm3, tn3, z); tile_of(4, bid, tm4, tn4, ks4);
  const __amdgpu_buffer_rsrc_t rx = mk_rsrc(P.x, S_ * D_ * 2), rws = mk_rsrc(ws, (unsigned)WS_FL);
  const __amdgpu_buffer_rsrc_t r1 = mk_rsrc(P.w1, D_ * D_ * 2), r2 = mk_rsrc(P.w2, D_ * D_ * 2), r3 = mk_rsrc(P.w3, D_ * F_ * 2), r4 = mk_rsrc(P.w4, D_ * F_ * 2);
  unsigned vx[4], v1[2], v2[2], v3[7], v4[4];
#pragma unroll
  for (int i = 0; i < 4; i++) { const int c = (lw * 4 + i) * 64 + l, r = c >> 3; vx[i] = (unsigned)((tm1 * 128 + r) * D_ * 2 + (((c & 7) ^ (r & 7)) << 4)); }
#pragma unroll
  for (int i = 0; i < 2; i++) { v1[i] = w_voff<1>(i, lw, l, tn1, 0); v2[i] = w_voff<2>(i, lw, l, tn1, 0); }
  const unsigned va = lw * 4096 + l * 16;
  const int rot = ((tn1 & 7) + 2 * (tm1 & 3)) & 15;
  const int rg = l / 7, w3col = tn1 * 224 + lw * 56 + (l - rg * 7) * 8;
  u32x4 pf[8], pw[7] = {}, pg[2] = {};
  const int aland = l == 0 ? C_LAND + lw * 4 : C_DUM + l * 4;
  sfor<0, PROG.n>([&](auto ic) {
    constexpr int K = PROG.kind[ic.value], s = PROG.s[ic.value], c = PROG.c[ic.value];
    constexpr int MODE = (K == O_POLL || K == O_SIDE || K == O_FIN) ? s : ph_of(s);
    LAS char* base = (LAS char*)sm + st_off(s);
    if constexpr (K == O_WAITC) lds_wait4<LSL>(sm, C_CONS, c);
    if constexpr (K == O_W) {
      if constexpr (s == 32) {
#pragma unroll
        for (int i = 0; i < 7; i++) v3[i] = w_voff<3>(i, lw, l, tn3, 0);
      }
      if constexpr (s == 48) {
#pragma unroll
        for (int i = 0; i < 4; i++) v4[i] = w_voff<4>(i, lw, l, tn4, ks4);
      }
      constexpr int NLB = st_nlb(s), t = s - ph_first(MODE);
      const unsigned so = (MODE <= 2 ? ((t + rot) & 15) : t) * (64 * Cfg<MODE>::LDW * 2);
#pragma unroll
      for (int i = 0; i < NLB; i++)
        __builtin_amdgcn_raw_ptr_buffer_load_lds(MODE == 1 ? r1 : MODE == 2 ? r2 : MODE == 3 ? r3 : r4, base + 16384 + (lw * NLB + i) * 1024, 16,
                                                 MODE == 1 ? v1[i & 1] : MODE == 2 ? v2[i & 1] : MODE == 3 ? v3[i] : v4[i & 3], so, 0, 0);
    }
    if constexpr (K == O_A) {
      constexpr int t = s - ph_first(MODE);
      const unsigned so = MODE == 1   ? ((t + rot) & 15) * 128
                          : MODE == 2 ? (unsigned)WS_G + (tm1 * 16 + ((t + rot) & 15)) * 16384
                          : MODE == 3 ? (unsigned)WS_X1 + (tm3 * 16 + t) * 16384
                                      : (unsigned)WS_H2 + (tm4 * 56 + ks4 * 28 + t) * 16384;
      sfor<0, 4>([&](auto jc) {
        constexpr int i = jc.value;
        if constexpr (MODE == 1) __builtin_amdgcn_raw_ptr_buffer_load_lds(rx, base + (lw * 4 + i) * 1024, 16, vx[i], so, 0, 0);
        else __builtin_amdgcn_raw_ptr_buffer_load_lds(rws, base + lw * 4096, 16, va, so, i * 1024, LDAUX);
      });
    }
    if constexpr (K == O_POLL) {
      if constexpr (MODE == 4) {
#pragma unroll
        for (int r = 0; r < 7; r++) asm volatile("" ::"v"(pw[r]));
      }
      ST(0);
      bar();
      ST(1);
    }
    if constexpr (K == O_LAND) {
      wait_vm<c>();
      asm volatile("" ::: "memory");
      bar();
    }
    if constexpr (K == O_SIDE) {
      __builtin_amdgcn_sched_barrier(0);
      if constexpr (MODE == 1) {
#pragma unroll
        for (int r = 0; r < 8; r++) pf[r] = __builtin_amdgcn_raw_buffer_load_b128(r3, ((tm1 * 64 + (rg & 7) * 8 + r) * F_ + w3col) * 2, 0, 0);
      }
      if constexpr (MODE == 4) {
#pragma unroll
        for (int r = 0; r < 2; r++) pg[r] = __builtin_amdgcn_raw_buffer_load_b128(r2, bid * 8192 + r * 4096 + lw * 1024 + l * 16, 0, 0);
      }
      if constexpr (MODE == 2) {
#pragma unroll
        for (int r = 0; r < 7; r++) pw[r] = __builtin_amdgcn_raw_buffer_load_b128(r4, bid * 28672 + r * 4096 + va / 4096 * 1024 + l * 16, 0, 0);
      }
      if constexpr (MODE == 3) {
#pragma unroll
        for (int r = 0; r < 4; r++)
          pf[r] = __builtin_amdgcn_raw_buffer_load_b128(rws, WS_CS + ((((l / 14) & 3) * 4 + r) * F_ + tn3 * 224 + lw * 56 + (l % 14) * 4) * 4, 0, 16);
#pragma unroll
        for (int r = 0; r < 4; r++) pf[4 + r] = __builtin_amdgcn_raw_buffer_load_b128(rws, WS_ST + (tm3 * 128 + lw * 32) * 128 + r * 1024 + l * 16, 0, 16);
      }
      asm volatile("" ::: "memory");
    }
    if constexpr (K == O_FIN && MODE == 1) {
      asm volatile("" ::"v"(pg[0]), "v"(pg[1]));
#pragma unroll
      for (int r = 0; r < 8; r++) asm volatile("" : "+v"(pf[r]));
      float a[8] = {0.f, 0.f, 0.f, 0.f, 0.f, 0.f, 0.f, 0.f};
#pragma unroll
      for (int r = 0; r < 8; r++)
#pragma unroll
        for (int e = 0; e < 4; e++) { a[2 * e] += lo16(pf[r][e]); a[2 * e + 1] += hi16(pf[r][e]); }
#pragma unroll
      for (int e = 0; e < 8; e++) {
        a[e] += __shfl_down(a[e], 28, 64); a[e] += __shfl_down(a[e], 14, 64); a[e] += __shfl_down(a[e], 7, 64);
      }
      if (l < 7) {
        __builtin_amdgcn_raw_buffer_store_b128(__builtin_bit_cast(u32x4, f32x4{a[0], a[1], a[2], a[3]}), rws, WS_CS + (tm1 * F_ + w3col) * 4, 0, 16);
        __builtin_amdgcn_raw_buffer_store_b128(__builtin_bit_cast(u32x4, f32x4{a[4], a[5], a[6], a[7]}), rws, WS_CS + (tm1 * F_ + w3col) * 4 + 16, 0, 16);
      }
      signal(sm, 4, flag(ws, 3, tn1 * 16 + tm1), tag);
    }
    if constexpr (K == O_FIN && MODE == 3) {
#pragma unroll
      for (int r = 0; r < 8; r++) asm volatile("" : "+v"(pf[r]));
      f32x4 v = (__builtin_bit_cast(f32x4, pf[0]) + __builtin_bit_cast(f32x4, pf[1])) + (__builtin_bit_cast(f32x4, pf[2]) + __builtin_bit_cast(f32x4, pf[3]));
#pragma unroll
      for (int e = 0; e < 4; e++) { v[e] += __shfl_down(v[e], 28, 64); v[e] += __shfl_down(v[e], 14, 64); }
      if (l < 14) *(LAS f32x4*)((LAS char*)sm + C_CS3 + (lw * 56 + l * 4) * 4) = v;
#pragma unroll
      for (int r = 0; r < 4; r++) {
        const f32x4 u = __builtin_bit_cast(f32x4, pf[4 + r]);
        float a = u[0] + u[2], b = u[1] + u[3];
#pragma unroll
        for (int m = 1; m < 8; m *= 2) { a += __shfl_xor(a, m, 64); b += __shfl_xor(b, m, 64); }
        const float mean = a * (1.f / D_), var = fmaxf(b * (1.f / D_) - mean * mean, 0.f);
        const float rs = __builtin_amdgcn_rsqf(var + 1e-5f);
        if ((l & 7) == 0) *(LAS f32x2*)((LAS char*)sm + C_RS + (lw * 32 + r * 8 + (l >> 3)) * 8) = f32x2{rs, -mean * rs};
      }
    }
  });
  if (bid == 0 && lw == 0) {
#pragma unroll
    for (int i = 0; i < 4; i++) poll(flag(ws, 0, i * 64 + l), tag);
    if (l == 0) __hip_atomic_store((GAS u64*)(ws + WS_FL), tag, __ATOMIC_RELAXED, __HIP_MEMORY_SCOPE_AGENT);
  }
}

template <int MODE> DEVI void compute(const Ptrs& P, char* sm, u64 tag) {
  using C = Cfg<MODE>;
  constexpr int BN = C::BN, NT = C::NT, TM = C::TM, TN = C::TN, S0 = C::S0;
  char* ws = P.ws;
  const int bid = blockIdx.x, l = threadIdx.x & 63, w = __builtin_amdgcn_readfirstlane(threadIdx.x >> 6), g = l >> 4, lt = l & 15;
  int tm, tn, ks;
  tile_of(MODE, bid, tm, tn, ks);
  const int wm = MODE <= 2 ? w : (w >> 1), wn = MODE <= 2 ? 0 : (w & 1);
  const int tg0 = tm * 8 + wm * TM;
  const int n0 = tn * BN + wn * TN * 16;
  const __amdgpu_buffer_rsrc_t rws = mk_rsrc(ws, (unsigned)WS_FL);
  const int acons = l == 0 ? C_CONS + w * 4 : C_DUM + l * 4;

  f32x4 acc[TM][TN];
#pragma unroll
  for (int i = 0; i < TM; i++)
#pragma unroll
    for (int j = 0; j < TN; j++) acc[i][j] = f32x4{0.f, 0.f, 0.f, 0.f};
  f32x4 cs = {0.f, 0.f, 0.f, 0.f}, sx[2], sq[2];
  sx[0] = sx[1] = sq[0] = sq[1] = cs;
  u32x2 xr[MODE == 2 ? TM : 1][MODE == 2 ? TN : 1];
  u32x4 x1r[MODE == 4 ? 2 : 1][MODE == 4 ? 2 : 1];
  if (MODE == 2) {
#pragma unroll
    for (int i = 0; i < TM; i++)
#pragma unroll
      for (int j = 0; j < TN; j++)
        xr[i][j] = *(const GAS u32x2*)(P.x + ((size_t)((tg0 + i) * 16 + lt) * D_ + n0 + j * 16 + g * 4) * 2);
  }
  unsigned oa[MODE == 1 ? 2 : 1], ob[TN];
  if (MODE == 1) {
    oa[0] = (wm * 32 + lt) * 128 + ((g ^ (lt & 7)) << 4);
    oa[MODE == 1 ? 1 : 0] = (wm * 32 + lt) * 128 + (((4 + g) ^ (lt & 7)) << 4);
  } else oa[0] = wm * TM * 2048 + l * 16;
  {
    const int rho = 4 * g + (lt >> 2), p = l & 3;
#pragma unroll
    for (int j = 0; j < TN; j++)
      ob[j] = (BN == 224 && j >= 2) ? ob[j & 1] + (j >> 1) * 64
                                    : 16384 + rho * BN * 2 + ((((MODE == 1 ? j ^ w : wn * TN + j) * 2) ^ swz<BN>(rho)) + (p >> 1)) * 16 + (p & 1) * 8;
  }
  bf16x8 fa[2][TM], fb[2][TN];
  constexpr int NA = MODE == 1 ? 2 : 1, NB = BN == 224 ? 2 : TN;
  const LAS char *ca[NA], *cb[NB];
  auto rd = [&](int st) {
#pragma unroll
    for (int j = 0; j < TN; j++) {
      const LAS char* q = (BN == 224 ? cb[(j & 1) % NB] + (j >> 1) * 64 : cb[j % NB]) + st * 32 * BN * 2;
      const s16x4 u = __builtin_amdgcn_ds_read_tr16_b64_v4i16((LAS s16x4*)q);
      const s16x4 v = __builtin_amdgcn_ds_read_tr16_b64_v4i16((LAS s16x4*)(q + 16 * BN * 2));
      fb[st][j] = __builtin_bit_cast(bf16x8, __builtin_shufflevector(u, v, 0, 1, 2, 3, 4, 5, 6, 7));
      if (j == 0) {
#pragma unroll
        for (int i = 0; i < TM; i++)
          fa[st][i] = MODE == 1 ? *(const LAS bf16x8*)(ca[st % NA] + i * 2048) : *(const LAS bf16x8*)(ca[0] + i * 2048 + st * 1024);
      }
    }
  };
  constexpr int RN = MODE <= 2 ? 6 : MODE == 3 ? 3 : NS4, RU = MODE == 4 && NS4 == 5 ? 32768 : 4096;
  unsigned seq = ring_seq(S0, RN, RU);
  auto adv = [&]() {
    const unsigned base = (seq & 31) * RU;
    seq = (seq >> 5) | ((seq & 31) << (5 * (RN - 1)));
#pragma unroll
    for (int i = 0; i < NA; i++) { ca[i] = (const LAS char*)sm + oa[i] + base; asm volatile("" : "+v"(ca[i])); }
#pragma unroll
    for (int j = 0; j < NB; j++) { cb[j] = (const LAS char*)sm + ob[j] + base; asm volatile("" : "+v"(cb[j])); }
  };
  const bf16x8 ones = {1.f, 1.f, 1.f, 1.f, 1.f, 1.f, 1.f, 1.f};
  auto mm = [&](int st) {
#pragma unroll
    for (int j = 0; j < TN; j++) {
#pragma unroll
      for (int i = 0; i < TM; i++) acc[i][j] = __builtin_amdgcn_mfma_f32_16x16x32_bf16(fb[st][j], fa[st][i], acc[i][j], 0, 0, 0);
    }
    if (MODE == 1) {
      cs = __builtin_amdgcn_mfma_f32_16x16x32_bf16(fb[st][0], ones, cs, 0, 0, 0);
#pragma unroll
      for (int i = 0; i < 2; i++) {
        sx[i] = __builtin_amdgcn_mfma_f32_16x16x32_bf16(ones, fa[st][i], sx[i], 0, 0, 0);
        sq[i] = __builtin_amdgcn_mfma_f32_16x16x32_bf16(fa[st][i], fa[st][i], sq[i], 0, 0, 0);
      }
    }
  };
  auto mix = [&](auto tl) {
    constexpr int T = tl.value, NR = TM + 2 * TN, NM = TM * TN + (MODE == 1 ? 5 : 0), NK = NM - 1 - (T ? TAILM : 0);
    constexpr int X2 = NR > NK ? NR - NK : 0, NG = NR - X2;
#pragma unroll
    for (int i = 0; i < NG; i++) {
      __builtin_amdgcn_sched_group_barrier(0x008, 1, 0);
      if (i < X2) __builtin_amdgcn_sched_group_barrier(0x100, 2, 0);
      else __builtin_amdgcn_sched_group_barrier(0x100, 1, 0);
    }
    if (T) {
      __builtin_amdgcn_sched_group_barrier(0x200, 1, 0);
      __builtin_amdgcn_sched_group_barrier(0x100, 1, 0);
      __builtin_amdgcn_sched_group_barrier(0x008, NM - NG - 2, 0);
      __builtin_amdgcn_sched_group_barrier(0x002, 3, 0);
      __builtin_amdgcn_sched_group_barrier(0x008, 2, 0);
    } else __builtin_amdgcn_sched_group_barrier(0x008, NM - NG, 0);
    __builtin_amdgcn_sched_barrier(0);
  };
  adv();
  ST(1);
  if (MODE > 1) bar();
  bar();
  ST(2);
  if (MODE == 4) {
#pragma unroll
    for (int i = 0; i < 2; i++)
#pragma unroll
      for (int j = 0; j < 2; j++)
        x1r[i][j] = __builtin_amdgcn_raw_buffer_load_b128(rws, WS_X1 + fo_block<D_>(tg0 + ks * 2 + i, (n0 >> 5) + j) + l * 16, 0, 16);
  }
  rd(0); rd(1); adv();
  __builtin_amdgcn_sched_barrier(0);
  mm(0);
  auto step = [&](bool sync) {
    __builtin_amdgcn_sched_barrier(0);
    if (sync) { wait_lgkm0(); bar(); }
    __builtin_amdgcn_sched_barrier(0);
    rd(0); mm(1); mix(std::integral_constant<int, 0>{});
    rd(1); adv(); mm(0); mix(std::integral_constant<int, 0>{});
  };
  if (MODE <= PAIR) {
    step(false);
    for (int t = 2; t < NT; t += 2) { step(true); step(false); }
  } else {
    for (int t = 1; t < NT; t++) step(true);
  }
  mm(1);
  ST(3);

  auto st16 = [&](unsigned off, u32x4 v) { __builtin_amdgcn_raw_buffer_store_b128(v, rws, off, 0, STAUX); };
  auto st8 = [&](unsigned off, u32x2 v) { __builtin_amdgcn_raw_buffer_store_b64(v, rws, off, 0, STAUX); };
  if (MODE == 1 || MODE == 3) {
    const int q = tn >> 2, sh = (q & 1) ? -1 - (q >> 1) : 1 + (q >> 1);
    const LAS char* csl = (const LAS char*)sm + (MODE == 1 ? C_CS1 : C_CS3);
    if (MODE == 1) {
      if (lt == 0) *(volatile LAS f32x4*)((LAS char*)sm + C_CS1 + (w * 16 + g * 4) * 4) = cs;
      lds_arrive(sm, C_CNT + 20);
#pragma nounroll
      for (int i = 0; i < SPIN && __builtin_amdgcn_readfirstlane(*(const volatile LAS unsigned*)((LAS char*)sm + C_CNT + 20)) < 4; i++) {}
      asm volatile("" ::: "memory");
    }
#pragma unroll
    for (int i = 0; i < TM; i++) {
      float rs, nm;
      if (MODE == 1) {
        const f32x4 d = sq[i & 1];
        const float a = sx[i & 1][0], b = __shfl((lt & 2) ? ((lt & 1) ? d[3] : d[2]) : ((lt & 1) ? d[1] : d[0]), (lt >> 2) * 16 + lt, 64);
        const float mean = a * (1.f / D_), var = fmaxf(b * (1.f / D_) - mean * mean, 0.f);
        rs = __builtin_amdgcn_rsqf(var + 1e-5f); nm = -mean * rs;
      } else {
        const f32x2 u = *(const LAS f32x2*)((const LAS char*)sm + C_RS + ((wm * TM + i) * 16 + lt) * 8);
        rs = u[0]; nm = u[1];
      }
      unsigned pk[TN][2];
#pragma unroll
      for (int j = 0; j < TN; j++) {
        const f32x4 c = *(const LAS f32x4*)(csl + ((MODE == 1 ? (j ^ w) * 16 : wn * 112 + j * 16) + g * 4) * 4);
        const f32x4 y = acc[i][j] * rs + c * nm;
        const f32x2 u = gelu2(f32x2{y[0], y[1]}), v = gelu2(f32x2{y[2], y[3]});
        pk[j][0] = pk2(u.x, u.y); pk[j][1] = pk2(v.x, v.y);
      }
      if (MODE == 1) {
        const int td = (tg0 + i) * 16 + lt + sh, tw = td & (S_ - 1);
        const unsigned keep = td == tw ? 0xffffffffu : 0u;
#pragma unroll
        for (int j = 0; j < TN; j += 2) {
          const bool sw = w & 1;
          const u32x4 v = {(sw ? pk[j + 1][0] : pk[j][0]) & keep, (sw ? pk[j + 1][1] : pk[j][1]) & keep,
                           (sw ? pk[j][0] : pk[j + 1][0]) & keep, (sw ? pk[j][1] : pk[j + 1][1]) & keep};
          st16(WS_G + fo_block<D_>(tw >> 4, (n0 >> 5) + ((j ^ w) >> 1)) + (g * 16 + (tw & 15)) * 16, v);
        }
      } else {
        const int kb0 = tn * 7 + wn * 4;
        if (wn == 0) {
#pragma unroll
          for (int j = 0; j < 3; j++)
            st16(WS_H2 + fo_block<F_>(tg0 + i, kb0 + j) + l * 16, u32x4{pk[2 * j][0], pk[2 * j][1], pk[2 * j + 1][0], pk[2 * j + 1][1]});
          st8(WS_H2 + fo_block<F_>(tg0 + i, kb0 + 3) + l * 16, u32x2{pk[TN - 1][0], pk[TN - 1][1]});
        } else {
          st8(WS_H2 + fo_block<F_>(tg0 + i, kb0 - 1) + l * 16 + 8, u32x2{pk[0][0], pk[0][1]});
#pragma unroll
          for (int j = 0; j < 3; j++)
            st16(WS_H2 + fo_block<F_>(tg0 + i, kb0 + j) + l * 16, u32x4{pk[(2 * j + 1) % TN][0], pk[(2 * j + 1) % TN][1], pk[(2 * j + 2) % TN][0], pk[(2 * j + 2) % TN][1]});
        }
      }
    }
  }
  if (MODE == 2) {
#pragma unroll
    for (int i = 0; i < TM; i++) {
      unsigned pk[TN][2];
      float a = 0.f, b = 0.f;
#pragma unroll
      for (int j = 0; j < TN; j++)
#pragma unroll
        for (int e = 0; e < 2; e++) {
          const unsigned h = pk2(acc[i][j][2 * e], acc[i][j][2 * e + 1]), x = xr[MODE == 2 ? i : 0][MODE == 2 ? j : 0][e];
          const unsigned y = pk2(lo16(x) + lo16(h), hi16(x) + hi16(h));
          pk[j][e] = y;
          a += lo16(y) + hi16(y); b += lo16(y) * lo16(y) + hi16(y) * hi16(y);
        }
#pragma unroll
      for (int j = 0; j < TN; j += 2)
        st16(WS_X1 + fo_block<D_>(tg0 + i, (n0 >> 5) + (j >> 1)) + l * 16, u32x4{pk[j][0], pk[j][1], pk[j + 1][0], pk[j + 1][1]});
      a += xor16(a); b += xor16(b); a += xor32(a); b += xor32(b);
      if (g == 0) st8(WS_ST + ((tg0 + i) * 16 + lt) * 128 + tn * 8, __builtin_bit_cast(u32x2, f32x2{a, b}));
    }
  }
  if (MODE <= 3) {
    ST(4);
    int um, un, uk;
    tile_of(MODE + 1, bid, um, un, uk);
    GAS u64* f = MODE == 1   ? flag(ws, 0, ((um + 15) * 16 + (l < 48 ? l : 16)) & 255)
                 : MODE == 2 ? (l < 32 ? flag(ws, 1, um * 16 + (l & 15)) : flag(ws, 3, un * 16 + (l & 15)))
                             : flag(ws, 2, um * 16 + uk * 8 + (l & 7));
    if (signal(sm, MODE - 1, flag(ws, MODE - 1, tm * 16 + tn), tag)) poll(f, tag);
  }
  if (MODE == 4) {
    const __amdgpu_buffer_rsrc_t ro = mk_rsrc(P.out, S_ * D_ * 2);
    const unsigned oo = ((tg0 * 16 + (l >> 3)) * D_ + n0) * 2 + (l & 7) * 16, og = WS_G + tm * 262144 + tn * 32768 + w * 4096 + l * 16;
    f32x4 mine[2][TN];
#pragma unroll
    for (int q = 0; q < 4; q++) {
      u32x4 hi, lo;
#pragma unroll
      for (int e = 0; e < 2; e++) {
        const int i = q >> 1, j = ((q & 1) * 2 + e) % TN;
        const u32x4 s = __builtin_bit_cast(u32x4, ks ? acc[i][j] : acc[(2 + i) % TM][j]);
        mine[i][j] = ks ? acc[(2 + i) % TM][j] : acc[i][j];
        hi[2 * e] = __builtin_amdgcn_perm(s[1], s[0], 0x07060302); hi[2 * e + 1] = __builtin_amdgcn_perm(s[3], s[2], 0x07060302);
        lo[2 * e] = __builtin_amdgcn_perm(s[1], s[0], 0x05040100); lo[2 * e + 1] = __builtin_amdgcn_perm(s[3], s[2], 0x05040100);
      }
      __builtin_amdgcn_raw_buffer_store_b128(hi, ro, oo + ((1 - ks) * 32 + q * 8) * D_ * 2, 0, 16);
      __builtin_amdgcn_raw_buffer_store_b128(lo, rws, og + ks * 16384 + q * 1024, 0, 16);
    }
    asm volatile("s_waitcnt vmcnt(0)" ::: "memory");
    if (l == 0) __hip_atomic_store(flag(ws, 4, bid * 4 + w), tag, __ATOMIC_RELAXED, __HIP_MEMORY_SCOPE_AGENT);
    poll(flag(ws, 4, (bid ^ 1) * 4 + w), tag);
    ST(4);
    u32x4 rh[4], rl[4], oth[2][TN];
#pragma unroll
    for (int q = 0; q < 4; q++) {
      rh[q] = __builtin_amdgcn_raw_buffer_load_b128(ro, oo + (ks * 32 + q * 8) * D_ * 2, 0, 16);
      rl[q] = __builtin_amdgcn_raw_buffer_load_b128(rws, og + (1 - ks) * 16384 + q * 1024, 0, 16);
    }
    asm volatile("s_waitcnt vmcnt(0)" ::: "memory");
#pragma unroll
    for (int q = 0; q < 4; q++)
#pragma unroll
      for (int e = 0; e < 2; e++)
        oth[q >> 1][((q & 1) * 2 + e) % TN] =
            u32x4{__builtin_amdgcn_perm(rh[q][2 * e], rl[q][2 * e], 0x05040100), __builtin_amdgcn_perm(rh[q][2 * e], rl[q][2 * e], 0x07060302),
                  __builtin_amdgcn_perm(rh[q][2 * e + 1], rl[q][2 * e + 1], 0x05040100), __builtin_amdgcn_perm(rh[q][2 * e + 1], rl[q][2 * e + 1], 0x07060302)};
#pragma unroll
    for (int i = 0; i < 2; i++)
#pragma unroll
      for (int jp = 0; jp < 2; jp++) {
        unsigned y[4];
#pragma unroll
        for (int e = 0; e < 4; e++) {
          const int j = (jp * 2 + (e >> 1)) % TN, r = (e & 1) * 2;
          const f32x4 o = __builtin_bit_cast(f32x4, oth[i][j]);
          const unsigned h = pk2(mine[i][j][r] + o[r], mine[i][j][r + 1] + o[r + 1]), x = x1r[MODE == 4 ? i : 0][MODE == 4 ? jp : 0][e];
          y[e] = pk2(lo16(x) + lo16(h), hi16(x) + hi16(h));
        }
        const u32x2 p0 = __builtin_amdgcn_permlane16_swap(y[0], y[2], false, false);
        const u32x2 p1 = __builtin_amdgcn_permlane16_swap(y[1], y[3], false, false);
        const int col = n0 + jp * 32 + (g & 1) * 16 + (g >> 1) * 8;
        __builtin_amdgcn_raw_buffer_store_b128(u32x4{p0[0], p1[0], p0[1], p1[1]}, ro, (((tg0 + ks * 2 + i) * 16 + lt) * D_ + col) * 2, 0, OAUX);
      }
  }
  ST(5);
}
#endif

__global__ __launch_bounds__(512, 2) void mixer_k(const char* __restrict__ x, const char* __restrict__ w1, const char* __restrict__ w2,
                                                  const char* __restrict__ w3, const char* __restrict__ w4, char* ws, char* out) {
#if defined(__HIP_DEVICE_COMPILE__)
  extern __shared__ __attribute__((aligned(16))) char sm[];
  constexpr int MODE = 1;
  ST(0);
  if (threadIdx.x < 16) lds_set(sm, CTL + threadIdx.x * 4, 0u);
  const u64 tag = __hip_atomic_load((GAS u64*)(ws + WS_FL), __ATOMIC_RELAXED, __HIP_MEMORY_SCOPE_AGENT) + 1;
  const Ptrs P = {x, w1, w2, w3, w4, ws, out};
  wait_lgkm0();
  if (threadIdx.x >= 256) loader(P, sm, tag);
  else { compute<1>(P, sm, tag); compute<2>(P, sm, tag); compute<3>(P, sm, tag); compute<4>(P, sm, tag); }
#endif
}

extern "C" void kernel_launch(void* const* d_in, const int* in_sizes, int n_in, void* d_out, int out_size, void* d_ws,
                              size_t ws_size, hipStream_t stream) {
  (void)hipFuncSetAttribute((const void*)mixer_k, hipFuncAttributeMaxDynamicSharedMemorySize, LDS_BYTES);
  mixer_k<<<256, 512, LDS_BYTES, stream>>>((const char*)d_in[0], (const char*)d_in[1], (const char*)d_in[2], (const char*)d_in[3],
                                           (const char*)d_in[4], (char*)d_ws, (char*)d_out);
}
```

```cpp
#include <hip/hip_runtime.h>
#include <hip/hip_bf16.h>
#include <type_traits>

typedef __attribute__((ext_vector_type(8))) __bf16 bf16x8;
typedef __attribute__((ext_vector_type(4))) __bf16 bf16x4;
typedef __attribute__((ext_vector_type(2))) __bf16 bf16x2;
typedef __attribute__((ext_vector_type(4))) short s16x4;
typedef __attribute__((ext_vector_type(8))) short s16x8;
typedef __attribute__((ext_vector_type(4))) float f32x4;
typedef __attribute__((ext_vector_type(2))) float f32x2;
typedef __attribute__((ext_vector_type(4))) unsigned u32x4;
typedef __attribute__((ext_vector_type(2))) unsigned u32x2;
#define LAS __attribute__((address_space(3)))
#define GAS __attribute__((address_space(1)))
#define DEVI __device__ __forceinline__

constexpr int S_ = 2048, D_ = 1024, F_ = 3584;
constexpr size_t WS_G = 0;
constexpr size_t WS_X1 = WS_G + (size_t)S_ * D_ * 2;
constexpr size_t WS_H2 = WS_X1 + (size_t)S_ * D_ * 2;
constexpr size_t WS_ST = WS_H2 + (size_t)S_ * F_ * 2;
constexpr size_t WS_CS = WS_ST + (size_t)S_ * 16 * 8;
constexpr size_t WS_EX = WS_CS + (size_t)16 * F_ * 4;
constexpr size_t WS_FL = WS_EX + (size_t)256 * 32768;

#define ST(i)
#define LDAUX 16
#define LSL 1
#define FSP 4352
#define FST 256
#define PSL 1
#define SIDE1 16
#define SIDE0 8
#define STAUX 16
#define OAUX 0
#define TCH 15
#define PAIR 2
#define NS4 4
#define SIDE2 32
#define TAILM 2
typedef unsigned long long u64;
template <int MODE> struct Cfg;
template <> struct Cfg<1> { static constexpr int BN = 64, NT = 16, LDW = 1024, TM = 2, TN = 4, KD = 1024, S0 = 0; };
template <> struct Cfg<2> { static constexpr int BN = 64, NT = 16, LDW = 1024, TM = 2, TN = 4, KD = 1024, S0 = 16; };
template <> struct Cfg<3> { static constexpr int BN = 224, NT = 16, LDW = 3584, TM = 4, TN = 7, KD = 1024, S0 = 32; };
template <> struct Cfg<4> { static constexpr int BN = 128, NT = 28, LDW = 1024, TM = 4, TN = 4, KD = 3584, S0 = 48; };
constexpr int NSTG = 76, CTL = 156 * 1024, C_LAND = CTL, C_CONS = CTL + 16, C_CNT = CTL + 32, C_CS1 = CTL + 256, C_CS3 = CTL + 512,
              C_DUM = CTL + 1536, C_RS = CTL + 2048, LDS_BYTES = 160 * 1024;
constexpr int ph_of(int s) { return s < 16 ? 1 : s < 32 ? 2 : s < 48 ? 3 : 4; }
constexpr int ph_first(int p) { return p == 1 ? 0 : p == 2 ? 16 : p == 3 ? 32 : p == 4 ? 48 : NSTG; }
constexpr int st_nlb(int s) { return s < 32 ? 2 : s < 48 ? 7 : 4; }
constexpr int st_size(int s) { return 16384 + st_nlb(s) * 4096; }
constexpr int st_off(int s) {
  return 1024 * (s < 32 ? (s % 6) * 24 : s < 48 ? ((s - 32) % 3 == 0 ? 48 : (s - 32) % 3 == 1 ? 92 : 4)
                                               : NS4 == 5 ? ((s - 48) % 5 == 0 ? 96 : (s - 48) % 5 == 4 ? 128 : ((s - 48) % 5 - 1) * 32)
                                               : ((s & 3) == 0 ? 92 : (s & 3) == 1 ? 124 : (s & 3) == 2 ? 16 : 48));
}
constexpr unsigned ring_seq(int s0, int n, int unit) {
  unsigned q = 0;
  for (int i = 0; i < n; i++) q |= (unsigned)(st_off(s0 + i) / unit) << (5 * i);
  return q;
}
constexpr int st_need(int s) {
  int n = 0;
  for (int v = 0; v < s; v++)
    if (st_off(v) < st_off(s) + st_size(s) && st_off(s) < st_off(v) + st_size(v)) n = v + 1;
  return n;
}
enum { O_WAITC, O_W, O_A, O_POLL, O_LAND, O_SIDE, O_FIN };
struct Prog { int n, kind[400], s[400], c[400]; };
constexpr Prog mk_prog() {
  Prog P{};
  int own[700] = {}, nl = 0, nW = 0, nA = 0, cons = 0;
  auto op = [&](int k, int s, int c) { P.kind[P.n] = k; P.s[P.n] = s; P.c[P.n] = c; P.n++; };
  auto ld = [&](int o, int k) { for (int i = 0; i < k; i++) own[nl++] = o; };
  auto free_ = [&](int s) {};
  auto iw = [&]() { free_(nW); op(O_W, nW, 0); ld(nW, st_nlb(nW)); nW++; };
  auto ia = [&]() { free_(nA); op(O_A, nA, 0); ld(nA, 4); if (nW == nA) iw(); nA++; };
  for (int u = 0; u < NSTG; u++) {
    const int p = ph_of(u), e = ph_first(p + 1);
    if (u == ph_first(p)) {
      if (p == (SIDE1 > 16 ? 3 : 2)) op(O_FIN, 1, 0);
      if (p > 1) op(O_POLL, p, 0);
      ia(); ia();
      if (p == 3) { op(O_SIDE, 3, 0); ld(-1, 8); }
    }
    if (p <= PAIR && (u & 1)) continue;
    int last = 0;
    for (int i = 0; i < nl; i++) if (own[i] == u || (p <= PAIR && own[i] == u + 1)) last = i;
    if (u == 34) op(O_FIN, 3, 0);
    op(O_LAND, u, nl - 1 - last);
    for (int k = 0; k < (p <= PAIR ? 4 : 2); k++) if (nA < e && st_need(nA) <= u) {
      ia();
      if (nA == SIDE1) { op(O_SIDE, 1, 0); ld(-1, 8); }
      if (nA == SIDE0) { op(O_SIDE, 4, 0); ld(-1, 2); }
      if (nA == SIDE2) { op(O_SIDE, 2, 0); ld(-1, 7); }
    }
    if (nA == e) { while (nW < NSTG && nW < ph_first(p + 2) && st_need(nW) <= u) iw(); }
  }
  return P;
}
constexpr Prog PROG = mk_prog();
#if defined(__HIP_DEVICE_COMPILE__)
DEVI __amdgpu_buffer_rsrc_t mk_rsrc(const void* p, unsigned bytes) {
  return __builtin_amdgcn_make_buffer_rsrc((void*)p, 0, bytes, 0x00020000);
}
template <int N> DEVI void wait_vm() { __builtin_amdgcn_s_waitcnt(0x0F70 | (N & 15) | ((N >> 4) << 14)); }
DEVI void wait_lgkm0() { __builtin_amdgcn_s_waitcnt(0xC07F); }
DEVI void bar() { asm volatile("s_barrier" ::: "memory"); }
DEVI unsigned pk2(float a, float b) {
  bf16x2 v = {(__bf16)a, (__bf16)b};
  return __builtin_bit_cast(unsigned, v);
}
DEVI float lo16(unsigned u) { return __builtin_bit_cast(float, u << 16); }
DEVI float hi16(unsigned u) { return __builtin_bit_cast(float, u & 0xffff0000u); }
DEVI float xor16(float v) { return __shfl_xor(v, 16, 64); }
DEVI float xor32(float v) { return __shfl_xor(v, 32, 64); }
DEVI f32x2 gelu2(f32x2 x) {
  const float c0 = -2.0f * 0.7978845608028654f * 1.4426950408889634f, c1 = c0 * 0.044715f;
  f32x2 z = x * (x * x * c1 + c0);
  f32x2 e = {__builtin_amdgcn_exp2f(z.x), __builtin_amdgcn_exp2f(z.y)};
  e = e + 1.0f;
  f32x2 r = {__builtin_amdgcn_rcpf(e.x), __builtin_amdgcn_rcpf(e.y)};
  return x * r;
}
template <int I, int N, class F> DEVI void sfor(F&& f) {
  if constexpr (I < N) { f(std::integral_constant<int, I>{}); sfor<I + 1, N>(f); }
}
template <int BN> DEVI int swz(int rho) {
  return BN == 64 ? 2 * ((rho >> 1) & 3) : BN == 128 ? 2 * (rho & 7) : 2 * ((rho >> 2) & 1);
}
template <int KD> DEVI unsigned fo_block(int tg, int kb) {
  return (unsigned)((((tg >> 3) * (KD / 64) + (kb >> 1)) * 16 + (tg & 7) * 2 + (kb & 1)) * 1024);
}

DEVI void tile_of(int mode, int bid, int& tm, int& tn, int& ks) {
  const int xcd = bid & 7, loc = bid >> 3;
  ks = 0;
  if (mode <= 2) { tm = (xcd >> 1) * 4 + (loc >> 3); tn = (xcd & 1) * 8 + (loc & 7); }
  else if (mode == 3) { tm = (xcd >> 2) * 8 + (loc >> 2); tn = (xcd & 3) * 4 + (loc & 3); }
  else { tm = (xcd >> 1) * 4 + (loc >> 3); tn = loc & 7; ks = xcd & 1; }
}
#define SPIN (1 << 20)
DEVI unsigned lds_min4(const char* sm, int off) {
  const u32x4 v = *(const volatile LAS u32x4*)((const LAS char*)sm + off);
  return __builtin_amdgcn_readfirstlane(min(min(v[0], v[1]), min(v[2], v[3])));
}
template <int SL> DEVI void lds_wait4(const char* sm, int off, unsigned n) {
#pragma nounroll
  for (int i = 0; i < SPIN && lds_min4(sm, off) < n; i++) { if (SL) __builtin_amdgcn_s_sleep(SL); }
  asm volatile("" ::: "memory");
}
DEVI void lds_set(char* sm, int off, unsigned v) { *(volatile LAS unsigned*)((LAS char*)sm + off) = v; }
DEVI unsigned lds_arrive(char* sm, int off) {
  unsigned r = 0;
  if ((threadIdx.x & 63) == 0) r = __hip_atomic_fetch_add((LAS unsigned*)((LAS char*)sm + off), 1u, __ATOMIC_RELAXED, __HIP_MEMORY_SCOPE_WORKGROUP);
  return __builtin_amdgcn_readfirstlane(r);
}
DEVI GAS u64* flag(char* ws, int k, int i) { return (GAS u64*)(ws + WS_FL + 4096 + (k * 64 + (i >> 4)) * FSP + (i & 15) * FST); }
DEVI void poll(GAS u64* p, u64 tag) {
#pragma nounroll
  for (int i = 0; i < SPIN; i++) {
    if (__all(__hip_atomic_load(p, __ATOMIC_RELAXED, __HIP_MEMORY_SCOPE_AGENT) == tag)) break;
    __builtin_amdgcn_s_sleep(PSL);
  }
  asm volatile("" ::: "memory");
}
DEVI bool signal(char* sm, int cnt, GAS u64* f, u64 tag) {
  asm volatile("s_waitcnt vmcnt(0)" ::: "memory");
  const bool last = lds_arrive(sm, C_CNT + cnt * 4) == 3;
  if (last && (threadIdx.x & 63) == 0) __hip_atomic_store(f, tag, __ATOMIC_RELAXED, __HIP_MEMORY_SCOPE_AGENT);
  return last;
}

struct Ptrs { const char *x, *w1, *w2, *w3, *w4; char *ws, *out; };

template <int MODE> DEVI unsigned w_voff(int i, int lw, int l, int tn, int ks) {
  using C = Cfg<MODE>;
  constexpr int BN = C::BN;
  const int c = (lw * (BN / 32) + i) * 64 + l, r = c / (BN / 8), p = c % (BN / 8);
  const int k = MODE == 1 ? (r & 32) + 8 * ((r >> 2) & 3) + 4 * ((r >> 4) & 1) + (r & 3) : r;
  return (unsigned)(((ks * C::NT * 64 + k) * C::LDW + tn * BN) * 2 + ((p ^ swz<BN>(r)) << 4));
}

DEVI void loader(const Ptrs& P, char* sm, u64 tag) {
  char* ws = P.ws;
  const int bid = blockIdx.x, l = threadIdx.x & 63, lw = __builtin_amdgcn_readfirstlane(threadIdx.x >> 6) - 4;
  int tm1, tn1, tm3, tn3, tm4, tn4, ks4, z;
  tile_of(1, bid, tm1, tn1, z); tile_of(3, bid, tm3, tn3, z); tile_of(4, bid, tm4, tn4, ks4);
  const __amdgpu_buffer_rsrc_t rx = mk_rsrc(P.x, S_ * D_ * 2), rws = mk_rsrc(ws, (unsigned)WS_FL);
  const __amdgpu_buffer_rsrc_t r1 = mk_rsrc(P.w1, D_ * D_ * 2), r2 = mk_rsrc(P.w2, D_ * D_ * 2), r3 = mk_rsrc(P.w3, D_ * F_ * 2), r4 = mk_rsrc(P.w4, D_ * F_ * 2);
  unsigned vx[4], v1[2], v2[2], v3[7], v4[4];
#pragma unroll
  for (int i = 0; i < 4; i++) { const int c = (lw * 4 + i) * 64 + l, r = c >> 3; vx[i] = (unsigned)((tm1 * 128 + r) * D_ * 2 + (((c & 7) ^ (r & 7)) << 4)); }
#pragma unroll
  for (int i = 0; i < 2; i++) { v1[i] = w_voff<1>(i, lw, l, tn1, 0); v2[i] = w_voff<2>(i, lw, l, tn1, 0); }
  const unsigned va = lw * 4096 + l * 16;
  const int rot = ((tn1 & 7) + 2 * (tm1 & 3)) & 15;
  const int rg = l / 7, w3col = tn1 * 224 + lw * 56 + (l - rg * 7) * 8;
  u32x4 pf[8], pw[7] = {}, pg[2] = {};
  const int aland = l == 0 ? C_LAND + lw * 4 : C_DUM + l * 4;
  sfor<0, PROG.n>([&](auto ic) {
    constexpr int K = PROG.kind[ic.value], s = PROG.s[ic.value], c = PROG.c[ic.value];
    constexpr int MODE = (K == O_POLL || K == O_SIDE || K == O_FIN) ? s : ph_of(s);
    LAS char* base = (LAS char*)sm + st_off(s);
    if constexpr (K == O_WAITC) lds_wait4<LSL>(sm, C_CONS, c);
    if constexpr (K == O_W) {
      if constexpr (s == 32) {
#pragma unroll
        for (int i = 0; i < 7; i++) v3[i] = w_voff<3>(i, lw, l, tn3, 0);
      }
      if constexpr (s == 48) {
#pragma unroll
        for (int i = 0; i < 4; i++) v4[i] = w_voff<4>(i, lw, l, tn4, ks4);
      }
      constexpr int NLB = st_nlb(s), t = s - ph_first(MODE);
      const unsigned so = (MODE <= 2 ? ((t + rot) & 15) : t) * (64 * Cfg<MODE>::LDW * 2);
#pragma unroll
      for (int i = 0; i < NLB; i++)
        __builtin_amdgcn_raw_ptr_buffer_load_lds(MODE == 1 ? r1 : MODE == 2 ? r2 : MODE == 3 ? r3 : r4, base + 16384 + (lw * NLB + i) * 1024, 16,
                                                 MODE == 1 ? v1[i & 1] : MODE == 2 ? v2[i & 1] : MODE == 3 ? v3[i] : v4[i & 3], so, 0, 0);
    }
    if constexpr (K == O_A) {
      constexpr int t = s - ph_first(MODE);
      const unsigned so = MODE == 1   ? ((t + rot) & 15) * 128
                          : MODE == 2 ? (unsigned)WS_G + (tm1 * 16 + ((t + rot) & 15)) * 16384
                          : MODE == 3 ? (unsigned)WS_X1 + (tm3 * 16 + t) * 16384
                                      : (unsigned)WS_H2 + (tm4 * 56 + ks4 * 28 + t) * 16384;
      sfor<0, 4>([&](auto jc) {
        constexpr int i = jc.value;
        if constexpr (MODE == 1) __builtin_amdgcn_raw_ptr_buffer_load_lds(rx, base + (lw * 4 + i) * 1024, 16, vx[i], so, 0, 0);
        else __builtin_amdgcn_raw_ptr_buffer_load_lds(rws, base + lw * 4096, 16, va, so, i * 1024, LDAUX);
      });
    }
    if constexpr (K == O_POLL) {
      if constexpr (MODE == 4) {
#pragma unroll
        for (int r = 0; r < 7; r++) asm volatile("" ::"v"(pw[r]));
      }
      ST(0);
      bar();
      ST(1);
    }
    if constexpr (K == O_LAND) {
      wait_vm<c>();
      asm volatile("" ::: "memory");
      bar();
    }
    if constexpr (K == O_SIDE) {
      __builtin_amdgcn_sched_barrier(0);
      if constexpr (MODE == 1) {
#pragma unroll
        for (int r = 0; r < 8; r++) pf[r] = __builtin_amdgcn_raw_buffer_load_b128(r3, ((tm1 * 64 + (rg & 7) * 8 + r) * F_ + w3col) * 2, 0, 0);
      }
      if constexpr (MODE == 4) {
#pragma unroll
        for (int r = 0; r < 2; r++) pg[r] = __builtin_amdgcn_raw_buffer_load_b128(r2, bid * 8192 + r * 4096 + lw * 1024 + l * 16, 0, 0);
      }
      if constexpr (MODE == 2) {
#pragma unroll
        for (int r = 0; r < 7; r++) pw[r] = __builtin_amdgcn_raw_buffer_load_b128(r4, bid * 28672 + r * 4096 + va / 4096 * 1024 + l * 16, 0, 0);
      }
      if constexpr (MODE == 3) {
#pragma unroll
        for (int r = 0; r < 4; r++)
          pf[r] = __builtin_amdgcn_raw_buffer_load_b128(rws, WS_CS + ((((l / 14) & 3) * 4 + r) * F_ + tn3 * 224 + lw * 56 + (l % 14) * 4) * 4, 0, 16);
#pragma unroll
        for (int r = 0; r < 4; r++) pf[4 + r] = __builtin_amdgcn_raw_buffer_load_b128(rws, WS_ST + (tm3 * 128 + lw * 32) * 128 + r * 1024 + l * 16, 0, 16);
      }
      asm volatile("" ::: "memory");
    }
    if constexpr (K == O_FIN && MODE == 1) {
      asm volatile("" ::"v"(pg[0]), "v"(pg[1]));
#pragma unroll
      for (int r = 0; r < 8; r++) asm volatile("" : "+v"(pf[r]));
      float a[8] = {0.f, 0.f, 0.f, 0.f, 0.f, 0.f, 0.f, 0.f};
#pragma unroll
      for (int r = 0; r < 8; r++)
#pragma unroll
        for (int e = 0; e < 4; e++) { a[2 * e] += lo16(pf[r][e]); a[2 * e + 1] += hi16(pf[r][e]); }
#pragma unroll
      for (int e = 0; e < 8; e++) {
        a[e] += __shfl_down(a[e], 28, 64); a[e] += __shfl_down(a[e], 14, 64); a[e] += __shfl_down(a[e], 7, 64);
      }
      if (l < 7) {
        __builtin_amdgcn_raw_buffer_store_b128(__builtin_bit_cast(u32x4, f32x4{a[0], a[1], a[2], a[3]}), rws, WS_CS + (tm1 * F_ + w3col) * 4, 0, 16);
        __builtin_amdgcn_raw_buffer_store_b128(__builtin_bit_cast(u32x4, f32x4{a[4], a[5], a[6], a[7]}), rws, WS_CS + (tm1 * F_ + w3col) * 4 + 16, 0, 16);
      }
      signal(sm, 4, flag(ws, 3, tn1 * 16 + tm1), tag);
    }
    if constexpr (K == O_FIN && MODE == 3) {
#pragma unroll
      for (int r = 0; r < 8; r++) asm volatile("" : "+v"(pf[r]));
      f32x4 v = (__builtin_bit_cast(f32x4, pf[0]) + __builtin_bit_cast(f32x4, pf[1])) + (__builtin_bit_cast(f32x4, pf[2]) + __builtin_bit_cast(f32x4, pf[3]));
#pragma unroll
      for (int e = 0; e < 4; e++) { v[e] += __shfl_down(v[e], 28, 64); v[e] += __shfl_down(v[e], 14, 64); }
      if (l < 14) *(LAS f32x4*)((LAS char*)sm + C_CS3 + (lw * 56 + l * 4) * 4) = v;
#pragma unroll
      for (int r = 0; r < 4; r++) {
        const f32x4 u = __builtin_bit_cast(f32x4, pf[4 + r]);
        float a = u[0] + u[2], b = u[1] + u[3];
#pragma unroll
        for (int m = 1; m < 8; m *= 2) { a += __shfl_xor(a, m, 64); b += __shfl_xor(b, m, 64); }
        const float mean = a * (1.f / D_), var = fmaxf(b * (1.f / D_) - mean * mean, 0.f);
        const float rs = __builtin_amdgcn_rsqf(var + 1e-5f);
        if ((l & 7) == 0) *(LAS f32x2*)((LAS char*)sm + C_RS + (lw * 32 + r * 8 + (l >> 3)) * 8) = f32x2{rs, -mean * rs};
      }
    }
  });
  if (bid == 0 && lw == 0) {
#pragma unroll
    for (int i = 0; i < 4; i++) poll(flag(ws, 0, i * 64 + l), tag);
    if (l == 0) __hip_atomic_store((GAS u64*)(ws + WS_FL), tag, __ATOMIC_RELAXED, __HIP_MEMORY_SCOPE_AGENT);
  }
}

template <int MODE> DEVI void compute(const Ptrs& P, char* sm, u64 tag) {
  using C = Cfg<MODE>;
  constexpr int BN = C::BN, NT = C::NT, TM = C::TM, TN = C::TN, S0 = C::S0;
  char* ws = P.ws;
  const int bid = blockIdx.x, l = threadIdx.x & 63, w = __builtin_amdgcn_readfirstlane(threadIdx.x >> 6), g = l >> 4, lt = l & 15;
  int tm, tn, ks;
  tile_of(MODE, bid, tm, tn, ks);
  const int wm = MODE <= 2 ? w : (w >> 1), wn = MODE <= 2 ? 0 : (w & 1);
  const int tg0 = tm * 8 + wm * TM;
  const int n0 = tn * BN + wn * TN * 16;
  const __amdgpu_buffer_rsrc_t rws = mk_rsrc(ws, (unsigned)WS_FL);
  const int acons = l == 0 ? C_CONS + w * 4 : C_DUM + l * 4;

  f32x4 acc[TM][TN];
#pragma unroll
  for (int i = 0; i < TM; i++)
#pragma unroll
    for (int j = 0; j < TN; j++) acc[i][j] = f32x4{0.f, 0.f, 0.f, 0.f};
  f32x4 cs = {0.f, 0.f, 0.f, 0.f}, sx[2], sq[2];
  sx[0] = sx[1] = sq[0] = sq[1] = cs;
  u32x2 xr[MODE == 2 ? TM : 1][MODE == 2 ? TN : 1];
  u32x4 x1r[MODE == 4 ? 2 : 1][MODE == 4 ? 2 : 1];
  if (MODE == 2) {
#pragma unroll
    for (int i = 0; i < TM; i++)
#pragma unroll
      for (int j = 0; j < TN; j++)
        xr[i][j] = *(const GAS u32x2*)(P.x + ((size_t)((tg0 + i) * 16 + lt) * D_ + n0 + j * 16 + g * 4) * 2);
  }
  unsigned oa[MODE == 1 ? 2 : 1], ob[TN];
  if (MODE == 1) {
    oa[0] = (wm * 32 + lt) * 128 + ((g ^ (lt & 7)) << 4);
    oa[MODE == 1 ? 1 : 0] = (wm * 32 + lt) * 128 + (((4 + g) ^ (lt & 7)) << 4);
  } else oa[0] = wm * TM * 2048 + l * 16;
  {
    const int rho = 4 * g + (lt >> 2), p = l & 3;
#pragma unroll
    for (int j = 0; j < TN; j++)
      ob[j] = (BN == 224 && j >= 2) ? ob[j & 1] + (j >> 1) * 64
                                    : 16384 + rho * BN * 2 + ((((MODE == 1 ? j ^ w : wn * TN + j) * 2) ^ swz<BN>(rho)) + (p >> 1)) * 16 + (p & 1) * 8;
  }
  bf16x8 fa[2][TM], fb[2][TN];
  constexpr int NA = MODE == 1 ? 2 : 1, NB = BN == 224 ? 2 : TN;
  const LAS char *ca[NA], *cb[NB];
  auto rd = [&](int st) {
#pragma unroll
    for (int j = 0; j < TN; j++) {
      const LAS char* q = (BN == 224 ? cb[(j & 1) % NB] + (j >> 1) * 64 : cb[j % NB]) + st * 32 * BN * 2;
      const s16x4 u = __builtin_amdgcn_ds_read_tr16_b64_v4i16((LAS s16x4*)q);
      const s16x4 v = __builtin_amdgcn_ds_read_tr16_b64_v4i16((LAS s16x4*)(q + 16 * BN * 2));
      fb[st][j] = __builtin_bit_cast(bf16x8, __builtin_shufflevector(u, v, 0, 1, 2, 3, 4, 5, 6, 7));
      if (j == 0) {
#pragma unroll
        for (int i = 0; i < TM; i++)
          fa[st][i] = MODE == 1 ? *(const LAS bf16x8*)(ca[st % NA] + i * 2048) : *(const LAS bf16x8*)(ca[0] + i * 2048 + st * 1024);
      }
    }
  };
  constexpr int RN = MODE <= 2 ? 6 : MODE == 3 ? 3 : NS4, RU = MODE == 4 && NS4 == 5 ? 32768 : 4096;
  unsigned seq = ring_seq(S0, RN, RU);
  auto adv = [&]() {
    const unsigned base = (seq & 31) * RU;
    seq = (seq >> 5) | ((seq & 31) << (5 * (RN - 1)));
#pragma unroll
    for (int i = 0; i < NA; i++) { ca[i] = (const LAS char*)sm + oa[i] + base; asm volatile("" : "+v"(ca[i])); }
#pragma unroll
    for (int j = 0; j < NB; j++) { cb[j] = (const LAS char*)sm + ob[j] + base; asm volatile("" : "+v"(cb[j])); }
  };
  const bf16x8 ones = {1.f, 1.f, 1.f, 1.f, 1.f, 1.f, 1.f, 1.f};
  auto mm = [&](int st) {
#pragma unroll
    for (int j = 0; j < TN; j++) {
#pragma unroll
      for (int i = 0; i < TM; i++) acc[i][j] = __builtin_amdgcn_mfma_f32_16x16x32_bf16(fb[st][j], fa[st][i], acc[i][j], 0, 0, 0);
    }
    if (MODE == 1) {
      cs = __builtin_amdgcn_mfma_f32_16x16x32_bf16(fb[st][0], ones, cs, 0, 0, 0);
#pragma unroll
      for (int i = 0; i < 2; i++) {
        sx[i] = __builtin_amdgcn_mfma_f32_16x16x32_bf16(ones, fa[st][i], sx[i], 0, 0, 0);
        sq[i] = __builtin_amdgcn_mfma_f32_16x16x32_bf16(fa[st][i], fa[st][i], sq[i], 0, 0, 0);
      }
    }
  };
  auto mix = [&](auto tl) {
    constexpr int T = tl.value, NR = TM + 2 * TN, NM = TM * TN + (MODE == 1 ? 5 : 0), NK = NM - 1 - (T ? TAILM : 0);
    constexpr int X2 = NR > NK ? NR - NK : 0, NG = NR - X2;
#pragma unroll
    for (int i = 0; i < NG; i++) {
      __builtin_amdgcn_sched_group_barrier(0x008, 1, 0);
      if (i < X2) __builtin_amdgcn_sched_group_barrier(0x100, 2, 0);
      else __builtin_amdgcn_sched_group_barrier(0x100, 1, 0);
    }
    if (T) {
      __builtin_amdgcn_sched_group_barrier(0x200, 1, 0);
      __builtin_amdgcn_sched_group_barrier(0x100, 1, 0);
      __builtin_amdgcn_sched_group_barrier(0x008, NM - NG - 2, 0);
      __builtin_amdgcn_sched_group_barrier(0x002, 3, 0);
      __builtin_amdgcn_sched_group_barrier(0x008, 2, 0);
    } else __builtin_amdgcn_sched_group_barrier(0x008, NM - NG, 0);
    __builtin_amdgcn_sched_barrier(0);
  };
  adv();
  ST(1);
  if (MODE > 1) bar();
  bar();
  ST(2);
  if (MODE == 4) {
#pragma unroll
    for (int i = 0; i < 2; i++)
#pragma unroll
      for (int j = 0; j < 2; j++)
        x1r[i][j] = __builtin_amdgcn_raw_buffer_load_b128(rws, WS_X1 + fo_block<D_>(tg0 + ks * 2 + i, (n0 >> 5) + j) + l * 16, 0, 16);
  }
  rd(0); rd(1); adv();
  __builtin_amdgcn_sched_barrier(0);
  mm(0);
  auto step = [&](bool sync) {
    __builtin_amdgcn_sched_barrier(0);
    if (sync) { wait_lgkm0(); bar(); }
    __builtin_amdgcn_sched_barrier(0);
    rd(0); mm(1); mix(std::integral_constant<int, 0>{});
    rd(1); adv(); mm(0); mix(std::integral_constant<int, 0>{});
  };
  if (MODE <= PAIR) {
    step(false);
    for (int t = 2; t < NT; t += 2) { step(true); step(false); }
  } else {
    for (int t = 1; t < NT; t++) step(true);
  }
  mm(1);
  ST(3);

  auto st16 = [&](unsigned off, u32x4 v) { __builtin_amdgcn_raw_buffer_store_b128(v, rws, off, 0, STAUX); };
  auto st8 = [&](unsigned off, u32x2 v) { __builtin_amdgcn_raw_buffer_store_b64(v, rws, off, 0, STAUX); };
  if (MODE == 1 || MODE == 3) {
    const int q = tn >> 2, sh = (q & 1) ? -1 - (q >> 1) : 1 + (q >> 1);
    const LAS char* csl = (const LAS char*)sm + (MODE == 1 ? C_CS1 : C_CS3);
    if (MODE == 1) {
      if (lt == 0) *(volatile LAS f32x4*)((LAS char*)sm + C_CS1 + (w * 16 + g * 4) * 4) = cs;
      lds_arrive(sm, C_CNT + 20);
#pragma nounroll
      for (int i = 0; i < SPIN && __builtin_amdgcn_readfirstlane(*(const volatile LAS unsigned*)((LAS char*)sm + C_CNT + 20)) < 4; i++) {}
      asm volatile("" ::: "memory");
    }
#pragma unroll
    for (int i = 0; i < TM; i++) {
      float rs, nm;
      if (MODE == 1) {
        const f32x4 d = sq[i & 1];
        const float a = sx[i & 1][0], b = __shfl((lt & 2) ? ((lt & 1) ? d[3] : d[2]) : ((lt & 1) ? d[1] : d[0]), (lt >> 2) * 16 + lt, 64);
        const float mean = a * (1.f / D_), var = fmaxf(b * (1.f / D_) - mean * mean, 0.f);
        rs = __builtin_amdgcn_rsqf(var + 1e-5f); nm = -mean * rs;
      } else {
        const f32x2 u = *(const LAS f32x2*)((const LAS char*)sm + C_RS + ((wm * TM + i) * 16 + lt) * 8);
        rs = u[0]; nm = u[1];
      }
      unsigned pk[TN][2];
#pragma unroll
      for (int j = 0; j < TN; j++) {
        const f32x4 c = *(const LAS f32x4*)(csl + ((MODE == 1 ? (j ^ w) * 16 : wn * 112 + j * 16) + g * 4) * 4);
        const f32x4 y = acc[i][j] * rs + c * nm;
        const f32x2 u = gelu2(f32x2{y[0], y[1]}), v = gelu2(f32x2{y[2], y[3]});
        pk[j][0] = pk2(u.x, u.y); pk[j][1] = pk2(v.x, v.y);
      }
      if (MODE == 1) {
        const int td = (tg0 + i) * 16 + lt + sh, tw = td & (S_ - 1);
        const unsigned keep = td == tw ? 0xffffffffu : 0u;
#pragma unroll
        for (int j = 0; j < TN; j += 2) {
          const bool sw = w & 1;
          const u32x4 v = {(sw ? pk[j + 1][0] : pk[j][0]) & keep, (sw ? pk[j + 1][1] : pk[j][1]) & keep,
                           (sw ? pk[j][0] : pk[j + 1][0]) & keep, (sw ? pk[j][1] : pk[j + 1][1]) & keep};
          st16(WS_G + fo_block<D_>(tw >> 4, (n0 >> 5) + ((j ^ w) >> 1)) + (g * 16 + (tw & 15)) * 16, v);
        }
      } else {
        const int kb0 = tn * 7 + wn * 4;
        if (wn == 0) {
#pragma unroll
          for (int j = 0; j < 3; j++)
            st16(WS_H2 + fo_block<F_>(tg0 + i, kb0 + j) + l * 16, u32x4{pk[2 * j][0], pk[2 * j][1], pk[2 * j + 1][0], pk[2 * j + 1][1]});
          st8(WS_H2 + fo_block<F_>(tg0 + i, kb0 + 3) + l * 16, u32x2{pk[TN - 1][0], pk[TN - 1][1]});
        } else {
          st8(WS_H2 + fo_block<F_>(tg0 + i, kb0 - 1) + l * 16 + 8, u32x2{pk[0][0], pk[0][1]});
#pragma unroll
          for (int j = 0; j < 3; j++)
            st16(WS_H2 + fo_block<F_>(tg0 + i, kb0 + j) + l * 16, u32x4{pk[(2 * j + 1) % TN][0], pk[(2 * j + 1) % TN][1], pk[(2 * j + 2) % TN][0], pk[(2 * j + 2) % TN][1]});
        }
      }
    }
  }
  if (MODE == 2) {
#pragma unroll
    for (int i = 0; i < TM; i++) {
      unsigned pk[TN][2];
      float a = 0.f, b = 0.f;
#pragma unroll
      for (int j = 0; j < TN; j++)
#pragma unroll
        for (int e = 0; e < 2; e++) {
          const unsigned h = pk2(acc[i][j][2 * e], acc[i][j][2 * e + 1]), x = xr[MODE == 2 ? i : 0][MODE == 2 ? j : 0][e];
          const unsigned y = pk2(lo16(x) + lo16(h), hi16(x) + hi16(h));
          pk[j][e] = y;
          a += lo16(y) + hi16(y); b += lo16(y) * lo16(y) + hi16(y) * hi16(y);
        }
#pragma unroll
      for (int j = 0; j < TN; j += 2)
        st16(WS_X1 + fo_block<D_>(tg0 + i, (n0 >> 5) + (j >> 1)) + l * 16, u32x4{pk[j][0], pk[j][1], pk[j + 1][0], pk[j + 1][1]});
      a += xor16(a); b += xor16(b); a += xor32(a); b += xor32(b);
      if (g == 0) st8(WS_ST + ((tg0 + i) * 16 + lt) * 128 + tn * 8, __builtin_bit_cast(u32x2, f32x2{a, b}));
    }
  }
  if (MODE <= 3) {
    ST(4);
    int um, un, uk;
    tile_of(MODE + 1, bid, um, un, uk);
    GAS u64* f = MODE == 1   ? flag(ws, 0, ((um + 15) * 16 + (l < 48 ? l : 16)) & 255)
                 : MODE == 2 ? (l < 32 ? flag(ws, 1, um * 16 + (l & 15)) : flag(ws, 3, un * 16 + (l & 15)))
                             : flag(ws, 2, um * 16 + uk * 8 + (l & 7));
    if (signal(sm, MODE - 1, flag(ws, MODE - 1, tm * 16 + tn), tag)) poll(f, tag);
  }
  if (MODE == 4) {
    const __amdgpu_buffer_rsrc_t ro = mk_rsrc(P.out, S_ * D_ * 2);
    const unsigned og = WS_G + tm * 262144 + tn * 32768 + w * 4096 + l * 16;
    f32x4 mine[2][TN];
#pragma unroll
    for (int q = 0; q < 4; q++) {
      u32x4 hi;
#pragma unroll
      for (int e = 0; e < 2; e++) {
        const int i = q >> 1, j = ((q & 1) * 2 + e) % TN;
        const u32x4 s = __builtin_bit_cast(u32x4, ks ? acc[i][j] : acc[(2 + i) % TM][j]);
        mine[i][j] = ks ? acc[(2 + i) % TM][j] : acc[i][j];
        { const f32x4 sf = __builtin_bit_cast(f32x4, s); hi[2 * e] = pk2(sf[0], sf[1]); hi[2 * e + 1] = pk2(sf[2], sf[3]); }
      }
      __builtin_amdgcn_raw_buffer_store_b128(hi, rws, og + ks * 16384 + q * 1024, 0, 16);
    }
    asm volatile("s_waitcnt vmcnt(0)" ::: "memory");
    if (l == 0) __hip_atomic_store(flag(ws, 4, bid * 4 + w), tag, __ATOMIC_RELAXED, __HIP_MEMORY_SCOPE_AGENT);
#pragma unroll
    for (int q = 0; q < 4; q++)
#pragma unroll
      for (int e = 0; e < 4; e++) {
        const int i = q >> 1, j = ((q & 1) * 2 + (e >> 1)) % TN, r = (e & 1) * 2;
        const unsigned x = x1r[MODE == 4 ? i : 0][MODE == 4 ? (q & 1) : 0][e];
        mine[i][j][r] += lo16(x); mine[i][j][r + 1] += hi16(x);
        asm volatile("" : "+v"(mine[i][j]));
      }
    poll(flag(ws, 4, (bid ^ 1) * 4 + w), tag);
    ST(4);
    u32x4 rh[4];
#pragma unroll
    for (int q = 0; q < 4; q++) {
      rh[q] = __builtin_amdgcn_raw_buffer_load_b128(rws, og + (1 - ks) * 16384 + q * 1024, 0, 16);
    }
#pragma unroll
    for (int i = 0; i < 2; i++)
#pragma unroll
      for (int jp = 0; jp < 2; jp++) {
        unsigned y[4];
#pragma unroll
        for (int e = 0; e < 4; e++) {
          const int j = (jp * 2 + (e >> 1)) % TN, r = (e & 1) * 2;
          const unsigned o = rh[i * 2 + jp][e];
          y[e] = pk2(mine[i][j][r] + lo16(o), mine[i][j][r + 1] + hi16(o));
        }
        const u32x2 p0 = __builtin_amdgcn_permlane16_swap(y[0], y[2], false, false);
        const u32x2 p1 = __builtin_amdgcn_permlane16_swap(y[1], y[3], false, false);
        const int col = n0 + jp * 32 + (g & 1) * 16 + (g >> 1) * 8;
        __builtin_amdgcn_raw_buffer_store_b128(u32x4{p0[0], p1[0], p0[1], p1[1]}, ro, (((tg0 + ks * 2 + i) * 16 + lt) * D_ + col) * 2, 0, OAUX);
      }
  }
  ST(5);
}
#endif

__global__ __launch_bounds__(512, 2) void mixer_k(const char* __restrict__ x, const char* __restrict__ w1, const char* __restrict__ w2,
                                                  const char* __restrict__ w3, const char* __restrict__ w4, char* ws, char* out) {
#if defined(__HIP_DEVICE_COMPILE__)
  extern __shared__ __attribute__((aligned(16))) char sm[];
  constexpr int MODE = 1;
  ST(0);
  if (threadIdx.x < 16) lds_set(sm, CTL + threadIdx.x * 4, 0u);
  const u64 tag = __hip_atomic_load((GAS u64*)(ws + WS_FL), __ATOMIC_RELAXED, __HIP_MEMORY_SCOPE_AGENT) + 1;
  const Ptrs P = {x, w1, w2, w3, w4, ws, out};
  wait_lgkm0();
  if (threadIdx.x >= 256) loader(P, sm, tag);
  else { compute<1>(P, sm, tag); compute<2>(P, sm, tag); compute<3>(P, sm, tag); compute<4>(P, sm, tag); }
#endif
}

extern "C" void kernel_launch(void* const* d_in, const int* in_sizes, int n_in, void* d_out, int out_size, void* d_ws,
                              size_t ws_size, hipStream_t stream) {
  (void)hipFuncSetAttribute((const void*)mixer_k, hipFuncAttributeMaxDynamicSharedMemorySize, LDS_BYTES);
  mixer_k<<<256, 512, LDS_BYTES, stream>>>((const char*)d_in[0], (const char*)d_in[1], (const char*)d_in[2], (const char*)d_in[3],
                                           (const char*)d_in[4], (char*)d_ws, (char*)d_out);
}
```
